```python
import torch

def _softmax(x, axis=-1):
    return torch.softmax(x, dim=axis)
def _gelu(x):
    return torch.nn.functional.gelu(x, approximate="tanh")
def _silu(x):
    return torch.nn.functional.silu(x)
def _sigmoid(x):
    return torch.sigmoid(x)
def _layernorm(x, axis=-1, eps=1e-5):
    m = x.mean(dim=axis, keepdim=True)
    v = x.var(dim=axis, keepdim=True, unbiased=False)
    return (x - m) / torch.sqrt(v + eps)
def _scatter_add(tgt, idx, src, axis=0):
    return tgt.clone().index_add_(axis, idx, src)
def _segment_sum(src, seg, n):
    out = torch.zeros((n,) + src.shape[1:], dtype=src.dtype)
    return out.index_add_(0, seg, src)
def _pad(x, pw):
    flat = tuple(v for pair in reversed(pw) for v in pair)
    return torch.nn.functional.pad(x, flat)

kernel_name = 'recurrent_scan__mha_block_cached__mixer_token_mlp__decode'

def setup_inputs():

    _w = (lambda a: (lambda t: t.to(torch.bfloat16) if t.is_floating_point() else t)(torch.as_tensor(a)))
    def _A_setup():
        B, Sq, D = 8, 1, 512
        H, Sk, Dh = 32, 8192, 128
        return {
            "x": _w((lambda *s: torch.randn(*s, dtype=torch.bfloat16) * 0.4)(B, Sq, D)),
            "k_cache": _w((lambda *s: torch.randn(*s, dtype=torch.bfloat16) * 1.6)(B, H, Sk, Dh)),
            "v_cache": _w((lambda *s: torch.randn(*s, dtype=torch.bfloat16) * 1.6)(B, H, Sk, Dh)),
            "wq": _w((lambda *s: torch.randn(*s, dtype=torch.bfloat16) * 1.6)(D, H * Dh) * (D ** -0.5)),
            "wo": _w((lambda *s: torch.randn(*s, dtype=torch.bfloat16) * 1.6)(H * Dh, D) * ((H * Dh) ** -0.5)),
        }
    _A_in = _A_setup()

    def _B_setup():
        B, S, D, F = 8, 1, 512, 1792
        return {
            "x": _w((lambda *s: torch.randn(*s, dtype=torch.bfloat16) * 0.4)(B, S, D)),
            "w1": _w((lambda *s: torch.randn(*s, dtype=torch.bfloat16) * 1.6)(S, F) * (S ** -0.5)),
            "b1": _w((lambda *s: torch.randn(*s, dtype=torch.bfloat16) * 1.6)(F) * 0.02),
            "w2": _w((lambda *s: torch.randn(*s, dtype=torch.bfloat16) * 1.6)(F, S) * (F ** -0.5)),
            "b2": _w((lambda *s: torch.randn(*s, dtype=torch.bfloat16) * 1.6)(S) * 0.02),
        }
    _B_in = _B_setup()

    return {
        "A_x": _A_in["x"],
        "A_k_cache": _A_in["k_cache"],
        "A_v_cache": _A_in["v_cache"],
        "A_wq": _A_in["wq"],
        "A_wo": _A_in["wo"],
        "B_x": _B_in["x"],
        "B_w1": _B_in["w1"],
        "B_b1": _B_in["b1"],
        "B_w2": _B_in["w2"],
        "B_b2": _B_in["b2"],
    }

def reference(A_x, A_k_cache, A_v_cache, A_wq, A_wo, B_x, B_w1, B_b1, B_w2, B_b2):
    def _A_ref(x, k_cache, v_cache, wq, wo):
        B, Sq, D = x.shape
        H, Dh = 32, 128
        q = (lambda x, *d: x.permute(*d))((x @ wq).reshape(B, Sq, H, Dh), 0, 2, 1, 3)
        scores = torch.einsum("bhqd,bhkd->bhqk", q, k_cache) * (Dh ** -0.5)
        p = _softmax(scores, axis=-1)
        o = torch.einsum("bhqk,bhkd->bhqd", p, v_cache)
        return (lambda x, *d: x.permute(*d))(o, 0, 2, 1, 3).reshape(B, Sq, H * Dh) @ wo

    def _B_ref(x, w1, b1, w2, b2):
        B, S, D = x.shape
        h = _layernorm(x, axis=-1)
        u = torch.einsum("bsd,sf->bdf", h, w1) + b1
        u = _gelu(u)
        y = torch.einsum("bdf,fs->bds", u, w2) + b2
        return (lambda x, *d: x.permute(*d))(y, 0, 2, 1)

    x = A_x
    xn = _layernorm(x, axis=-1)
    s = _A_ref(xn, A_k_cache, A_v_cache, A_wq, A_wo)
    g = _silu(_B_ref(xn, B_w1, B_b1, B_w2, B_b2))
    return x + s * g
```

```cpp
#include <hip/hip_runtime.h>
#include <hip/hip_bf16.h>
#include <stdint.h>

using u32x4 = __attribute__((ext_vector_type(4))) unsigned;
using f32x4 = __attribute__((ext_vector_type(4))) float;
using bf16x2 = __attribute__((ext_vector_type(2))) __bf16;

constexpr int NB = 8, DM = 512, NH = 32, SK = 8192, DH = 128, FF = 1792;

__device__ __forceinline__ float bf_lo(unsigned u) { return __builtin_bit_cast(float, u << 16); }
__device__ __forceinline__ float bf_hi(unsigned u) { return __builtin_bit_cast(float, u & 0xffff0000u); }
__device__ __forceinline__ float bf2f(unsigned short u) { return __builtin_bit_cast(float, (unsigned)u << 16); }
__device__ __forceinline__ float rbf(float x) { return (float)(__bf16)x; }
__device__ __forceinline__ unsigned short f2bf(float x) { return __builtin_bit_cast(unsigned short, (__bf16)x); }
__device__ __forceinline__ float dot2(unsigned k, unsigned q, float acc) {
  return __builtin_amdgcn_fdot2_f32_bf16(__builtin_bit_cast(bf16x2, k), __builtin_bit_cast(bf16x2, q), acc, false);
}
template <int CTRL> __device__ __forceinline__ float dpp(float x) {
  return __builtin_bit_cast(float, __builtin_amdgcn_mov_dpp(__builtin_bit_cast(int, x), CTRL, 0xf, 0xf, true));
}
template <int OFF> __device__ __forceinline__ void ldnt(u32x4& r, unsigned voff, const char* base) {
  asm volatile("global_load_dwordx4 %0, %1, %2 offset:%3 nt" : "+v"(r) : "v"(voff), "s"(base), "i"(OFF));
}
template <int N> __device__ __forceinline__ void waitv(u32x4& r) { asm volatile("s_waitcnt vmcnt(%1)" : "+v"(r) : "i"(N)); }
__device__ __forceinline__ float wave_sum(float v) {
#pragma unroll
  for (int o = 32; o > 0; o >>= 1) v += __shfl_xor(v, o);
  return v;
}
__device__ __forceinline__ void ln8(const float (&x)[8], float (&y)[8]) {
  float s = 0.f;
#pragma unroll
  for (int i = 0; i < 8; i++) s += x[i];
  const float mean = wave_sum(s) * (1.f / DM);
  float q = 0.f;
#pragma unroll
  for (int i = 0; i < 8; i++) { const float d = x[i] - mean; q += d * d; }
  const float var = rbf(wave_sum(q) * (1.f / DM));
  const float mb = rbf(mean);
  const float sd = rbf(sqrtf(rbf(var + 1e-5f)));
#pragma unroll
  for (int i = 0; i < 8; i++) y[i] = rbf(rbf(x[i] - mb) / sd);
}

struct Tile { u32x4 k[8], v[8]; };

template <bool LAST>
__device__ __forceinline__ void tile_step(Tile& t, const u32x4& q, float (&acc)[8], float& m, float& l, unsigned voff,
                                          const char* kn, const char* vn, int c) {
  float x[8];
#define QK(i)                                                                    \
  { waitv<LAST ? 15 - (i) : 15>(t.k[i]);                                         \
    float d = dot2(t.k[i][0], q[0], 0.f); d = dot2(t.k[i][1], q[1], d);          \
    d = dot2(t.k[i][2], q[2], d); d = dot2(t.k[i][3], q[3], d);                  \
    x[i] = d; asm volatile("" : "+v"(x[i]));                                     \
    if (!LAST) ldnt<(i) * 1024 - 4096>(t.k[i], voff, kn); }
  QK(0) QK(1) QK(2) QK(3) QK(4) QK(5) QK(6) QK(7)
#undef QK
  const bool b3 = c & 8, b2 = c & 4, b1 = c & 2;
  float y[4], z[2];
#pragma unroll
  for (int j = 0; j < 4; j++) y[j] = (b3 ? x[j + 4] : x[j]) + dpp<0x128>(b3 ? x[j] : x[j + 4]);
#pragma unroll
  for (int j = 0; j < 2; j++) z[j] = (b2 ? y[j + 2] : y[j]) + dpp<0x141>(b2 ? y[j] : y[j + 2]);
  float s = (b1 ? z[1] : z[0]) + dpp<0x4E>(b1 ? z[0] : z[1]);
  s += dpp<0xB1>(s);
  s = rbf(rbf(s) * 0.08838834764831845f) * 1.4426950408889634f;
  float mx = fmaxf(s, dpp<0x128>(s));
  mx = fmaxf(mx, dpp<0x124>(mx));
  mx = fmaxf(mx, dpp<0x122>(mx));
  const float mn = fmaxf(m, mx);
  const float alpha = __builtin_amdgcn_exp2f(m - mn);
  const float p = __builtin_amdgcn_exp2f(s - mn);
  l = fmaf(l, alpha, p);
  m = mn;
#pragma unroll
  for (int k = 0; k < 8; k++) acc[k] *= alpha;
#define PV(i)                                                                    \
  { waitv<LAST ? 7 - (i) : 15>(t.v[i]);                                          \
    const float pb = dpp<0x150 + 2 * (i)>(p);                                    \
    _Pragma("unroll") for (int k = 0; k < 4; k++) {                              \
      acc[2 * k] = fmaf(pb, bf_lo(t.v[i][k]), acc[2 * k]);                       \
      acc[2 * k + 1] = fmaf(pb, bf_hi(t.v[i][k]), acc[2 * k + 1]); }             \
    _Pragma("unroll") for (int k = 0; k < 8; k++) asm volatile("" : "+v"(acc[k])); \
    if (!LAST) ldnt<(i) * 1024 - 4096>(t.v[i], voff, vn); }
  PV(0) PV(1) PV(2) PV(3) PV(4) PV(5) PV(6) PV(7)
#undef PV
}

constexpr int SW = 4;
__global__ void __launch_bounds__(512, 2)
attn_main(const unsigned short* __restrict__ x, const char* __restrict__ K, const char* __restrict__ V,
          const unsigned short* __restrict__ wq, const unsigned short* __restrict__ wo,
          const unsigned short* __restrict__ w1, const unsigned short* __restrict__ b1,
          const unsigned short* __restrict__ w2, const unsigned short* __restrict__ b2,
          float* __restrict__ ypart, float* __restrict__ gate) {
  __shared__ __attribute__((aligned(16))) float lds[512 + 512 + 8 * 128 + 64 + 16 * 132 + 128 + 8 * 512];
  float* s_xn = lds;
  float* s_h = lds + 512;
  float* s_qp = lds + 1024;
  unsigned* s_q = (unsigned*)(lds + 2048);
  float* s_mrg = lds + 2048 + 64;
  float* s_o = s_mrg + 16 * 132;
  float* s_y = s_o + 128;
  const int tid = threadIdx.x, lane = tid & 63, wave = __builtin_amdgcn_readfirstlane(tid >> 6);
  const int r = lane >> 4, c = lane & 15;
  const int blk = blockIdx.x;
  const int h = (blk & 7) * 4 + ((blk >> 3) & 3), b = blk >> 5;
  const size_t slab = (size_t)(b * NH + h) * (SK * DH * 2);
  const unsigned voff = lane * 16;

  u32x4 wreg[16];
  {
    const unsigned short* wp = wq + (size_t)(wave * 64 + r) * (NH * DH) + h * DH + c * 8;
#pragma unroll
    for (int i = 0; i < 16; i++) wreg[i] = *(const u32x4*)(wp + (size_t)i * 4 * (NH * DH));
  }
  float xv[8], xn[8];
  {
    const u32x4 xr = *(const u32x4*)(x + b * DM + lane * 8);
#pragma unroll
    for (int k = 0; k < 4; k++) { xv[2 * k] = bf_lo(xr[k]); xv[2 * k + 1] = bf_hi(xr[k]); }
  }
  ln8(xv, xn);
  if (wave == 0) {
    *(f32x4*)(s_xn + lane * 8) = f32x4{xn[0], xn[1], xn[2], xn[3]};
    *(f32x4*)(s_xn + lane * 8 + 4) = f32x4{xn[4], xn[5], xn[6], xn[7]};
  }
  if (wave == 4) {
    float hh[8];
    ln8(xn, hh);
    *(f32x4*)(s_h + lane * 8) = f32x4{hh[0], hh[1], hh[2], hh[3]};
    *(f32x4*)(s_h + lane * 8 + 4) = f32x4{hh[4], hh[5], hh[6], hh[7]};
  }
  __syncthreads();
  {
    float qa[8] = {0, 0, 0, 0, 0, 0, 0, 0};
#pragma unroll
    for (int i = 0; i < 16; i++) {
      const float xs = s_xn[wave * 64 + i * 4 + r];
#pragma unroll
      for (int k = 0; k < 4; k++) { qa[2 * k] = fmaf(xs, bf_lo(wreg[i][k]), qa[2 * k]); qa[2 * k + 1] = fmaf(xs, bf_hi(wreg[i][k]), qa[2 * k + 1]); }
    }
#pragma unroll
    for (int k = 0; k < 8; k++) { qa[k] += __shfl_xor(qa[k], 16); qa[k] += __shfl_xor(qa[k], 32); }
    if (r == 0) {
      *(f32x4*)(s_qp + wave * 128 + c * 8) = f32x4{qa[0], qa[1], qa[2], qa[3]};
      *(f32x4*)(s_qp + wave * 128 + c * 8 + 4) = f32x4{qa[4], qa[5], qa[6], qa[7]};
    }
  }
  Tile t;
  const char* kp = K + slab + (size_t)wave * 8192 + 4096;
  const char* vp = V + slab + (size_t)wave * 8192 + 4096;
  if (wave < SW) {
    const u32x4 z4 = {0, 0, 0, 0};
#pragma unroll
    for (int i = 0; i < 8; i++) { t.k[i] = z4; t.v[i] = z4; }
    ldnt<-4096>(t.k[0], voff, kp); ldnt<-3072>(t.k[1], voff, kp); ldnt<-2048>(t.k[2], voff, kp); ldnt<-1024>(t.k[3], voff, kp);
    ldnt<0>(t.k[4], voff, kp); ldnt<1024>(t.k[5], voff, kp); ldnt<2048>(t.k[6], voff, kp); ldnt<3072>(t.k[7], voff, kp);
    ldnt<-4096>(t.v[0], voff, vp); ldnt<-3072>(t.v[1], voff, vp); ldnt<-2048>(t.v[2], voff, vp); ldnt<-1024>(t.v[3], voff, vp);
    ldnt<0>(t.v[4], voff, vp); ldnt<1024>(t.v[5], voff, vp); ldnt<2048>(t.v[6], voff, vp); ldnt<3072>(t.v[7], voff, vp);
  }
  __syncthreads();
  if (tid < 64) {
    float a0 = 0.f, a1 = 0.f;
#pragma unroll
    for (int w = 0; w < 8; w++) { a0 += s_qp[w * 128 + 2 * tid]; a1 += s_qp[w * 128 + 2 * tid + 1]; }
    s_q[tid] = (unsigned)f2bf(a0) | ((unsigned)f2bf(a1) << 16);
  }
  __syncthreads();
  if (wave < SW) {
    const u32x4 q = *(const u32x4*)(s_q + c * 4);
    float acc[8] = {0, 0, 0, 0, 0, 0, 0, 0};
    float m = -INFINITY, l = 0.f;
    m = -3.0e38f;
    constexpr int NT = SK / 32 / SW;
    for (int it = 0; it < NT - 1; it++) {
      kp += SW * 8192; vp += SW * 8192;
      tile_step<false>(t, q, acc, m, l, voff, kp, vp, c);
    }
    tile_step<true>(t, q, acc, m, l, voff, kp, vp, c);
    l += dpp<0x128>(l); l += dpp<0x124>(l); l += dpp<0x122>(l);
    float* rec = s_mrg + (wave * 4 + r) * 132;
    *(f32x4*)(rec + 4 + c * 8) = f32x4{acc[0], acc[1], acc[2], acc[3]};
    *(f32x4*)(rec + 8 + c * 8) = f32x4{acc[4], acc[5], acc[6], acc[7]};
    if (c == 0) { rec[0] = m; rec[1] = l; }
  } else {
    float hv[4], ya[4];
#pragma unroll
    for (int j = 0; j < 4; j++) { hv[j] = s_h[h * 16 + (wave - 4) * 4 + j]; ya[j] = 0.f; }
    for (int f = lane; f < FF; f += 64) {
      const float a = bf2f(w1[f]), bb = bf2f(b1[f]), w = bf2f(w2[f]);
#pragma unroll
      for (int j = 0; j < 4; j++) {
        const float u = rbf(rbf(hv[j] * a) + bb);
        const float zz = u * fmaf(u * u, 0.044715f, 1.f) * (-2.f * 0.7978845608028654f * 1.4426950408889634f);
        const float g = rbf(u * __builtin_amdgcn_rcpf(1.f + __builtin_amdgcn_exp2f(zz)));
        ya[j] = fmaf(g, w, ya[j]);
      }
    }
#pragma unroll
    for (int j = 0; j < 4; j++) {
      const float yy = rbf(rbf(wave_sum(ya[j])) + bf2f(b2[0]));
      const float g = rbf(yy / (1.f + __expf(-yy)));
      if (lane == 0) gate[b * DM + h * 16 + (wave - 4) * 4 + j] = g;
    }
  }
  u32x4 oreg[16];
  {
    const unsigned short* wp = wo + (size_t)(h * DH + wave * 16) * DM + lane * 8;
#pragma unroll
    for (int i = 0; i < 16; i++) oreg[i] = *(const u32x4*)(wp + i * DM);
  }
  __syncthreads();
  if (tid < 128) {
    float M = -3.0e38f;
#pragma unroll
    for (int i = 0; i < 16; i++) M = fmaxf(M, s_mrg[i * 132]);
    float L = 0.f, o = 0.f;
#pragma unroll
    for (int i = 0; i < 16; i++) {
      const float w = __builtin_amdgcn_exp2f(s_mrg[i * 132] - M);
      L = fmaf(w, s_mrg[i * 132 + 1], L);
      o = fmaf(w, s_mrg[i * 132 + 4 + tid], o);
    }
    s_o[tid] = rbf(o / L);
  }
  __syncthreads();
  {
    float ya[8] = {0, 0, 0, 0, 0, 0, 0, 0};
#pragma unroll
    for (int i = 0; i < 16; i++) {
      const float os = s_o[wave * 16 + i];
#pragma unroll
      for (int k = 0; k < 4; k++) { ya[2 * k] = fmaf(os, bf_lo(oreg[i][k]), ya[2 * k]); ya[2 * k + 1] = fmaf(os, bf_hi(oreg[i][k]), ya[2 * k + 1]); }
    }
    *(f32x4*)(s_y + wave * 512 + lane * 8) = f32x4{ya[0], ya[1], ya[2], ya[3]};
    *(f32x4*)(s_y + wave * 512 + lane * 8 + 4) = f32x4{ya[4], ya[5], ya[6], ya[7]};
  }
  __syncthreads();
  {
    float a = 0.f;
#pragma unroll
    for (int w = 0; w < 8; w++) a += s_y[w * 512 + tid];
    ypart[(size_t)(b * NH + h) * DM + tid] = a;
  }
}

__global__ void __launch_bounds__(256)
final_k(const unsigned short* __restrict__ x, const float* __restrict__ ypart, const float* __restrict__ gate, unsigned short* __restrict__ out) {
  __shared__ float red[256];
  const int b = blockIdx.x >> 3, col = (blockIdx.x & 7) * 64 + (threadIdx.x & 63), hq = threadIdx.x >> 6;
  float a = 0.f;
#pragma unroll
  for (int i = 0; i < 8; i++) a += ypart[(size_t)(b * NH + hq * 8 + i) * DM + col];
  red[threadIdx.x] = a;
  __syncthreads();
  if (threadIdx.x < 64) {
    const float s = rbf(red[threadIdx.x] + red[threadIdx.x + 64] + red[threadIdx.x + 128] + red[threadIdx.x + 192]);
    out[b * DM + col] = f2bf(bf2f(x[b * DM + col]) + rbf(s * gate[b * DM + col]));
  }
}

extern "C" void kernel_launch(void* const* d_in, const int* in_sizes, int n_in, void* d_out, int out_size, void* d_ws,
                              size_t ws_size, hipStream_t stream) {
  float* ypart = (float*)d_ws;
  float* gate = ypart + NB * NH * DM;
  attn_main<<<256, 512, 0, stream>>>((const unsigned short*)d_in[0], (const char*)d_in[1], (const char*)d_in[2],
                                     (const unsigned short*)d_in[3], (const unsigned short*)d_in[4],
                                     (const unsigned short*)d_in[6], (const unsigned short*)d_in[7],
                                     (const unsigned short*)d_in[8], (const unsigned short*)d_in[9], ypart, gate);
  final_k<<<64, 256, 0, stream>>>((const unsigned short*)d_in[0], ypart, gate, (unsigned short*)d_out);
}
```

```cpp
#include <hip/hip_runtime.h>
#include <hip/hip_bf16.h>
#include <stdint.h>
#include <type_traits>

using u32x4 = __attribute__((ext_vector_type(4))) unsigned;
using u32x2 = __attribute__((ext_vector_type(2))) unsigned;
using f32x4 = __attribute__((ext_vector_type(4))) float;
using f32x2 = __attribute__((ext_vector_type(2))) float;
using bf16x2 = __attribute__((ext_vector_type(2))) __bf16;
typedef unsigned long long u64;

constexpr int NB = 8, DM = 512, NH = 32, SK = 8192, DH = 128, FF = 1792;
constexpr float LOG2E = 1.4426950408889634f;

constexpr size_t WS_EPOCH = 0;
constexpr size_t WS_VOTE = 4096;
constexpr size_t WS_ML = 8192;
constexpr size_t WS_Q = 256 << 10;
constexpr size_t WS_S = 4 << 20;
constexpr size_t WS_Y = 8 << 20;

__device__ __forceinline__ float bf_lo(unsigned u) { return __builtin_bit_cast(float, u << 16); }
__device__ __forceinline__ float bf_hi(unsigned u) { return __builtin_bit_cast(float, u & 0xffff0000u); }
__device__ __forceinline__ float bf2f(unsigned short u) { return __builtin_bit_cast(float, (unsigned)u << 16); }
__device__ __forceinline__ float rbf(float x) { return (float)(__bf16)x; }
__device__ __forceinline__ unsigned short f2bf(float x) { return __builtin_bit_cast(unsigned short, (__bf16)x); }
__device__ __forceinline__ unsigned pack2(float a, float b) { return (unsigned)f2bf(a) | ((unsigned)f2bf(b) << 16); }
__device__ __forceinline__ float dot2(unsigned k, unsigned q, float acc) {
  return __builtin_amdgcn_fdot2_f32_bf16(__builtin_bit_cast(bf16x2, k), __builtin_bit_cast(bf16x2, q), acc, false);
}
template <int CTRL> __device__ __forceinline__ float dpp(float x) {
  return __builtin_bit_cast(float, __builtin_amdgcn_mov_dpp(__builtin_bit_cast(int, x), CTRL, 0xf, 0xf, true));
}
template <int OFF> __device__ __forceinline__ void ldnt(u32x4& r, unsigned voff, const char* base) {
  asm volatile("global_load_dwordx4 %0, %1, %2 offset:%3 nt" : "+v"(r) : "v"(voff), "s"(base), "i"(OFF));
}
__device__ __forceinline__ void ldpl(u32x4& r, unsigned voff, const char* base) {
  asm volatile("global_load_dwordx4 %0, %1, %2" : "+v"(r) : "v"(voff), "s"(base));
}
__device__ __forceinline__ void ld16(u32x4& r, unsigned voff, const void* base) {
  asm volatile("global_load_dwordx4 %0, %1, %2" : "=v"(r) : "v"(voff), "s"(base));
}
__device__ __forceinline__ void ld4(unsigned& r, unsigned voff, const void* base) {
  asm volatile("global_load_dword %0, %1, %2" : "=v"(r) : "v"(voff), "s"(base));
}
template <int N> __device__ __forceinline__ void waitv(unsigned& r) { asm volatile("s_waitcnt vmcnt(%1)" : "+v"(r) : "i"(N)); }
template <int N> __device__ __forceinline__ void waitv(u32x4& r) { asm volatile("s_waitcnt vmcnt(%1)" : "+v"(r) : "i"(N)); }
__device__ __forceinline__ void st16_dev(char* base, unsigned voff, u32x4 v) {
  asm volatile("global_store_dwordx4 %0, %1, %2 sc1\n\ts_nop 1" ::"v"(voff), "v"(v), "s"(base) : "memory");
}
__device__ __forceinline__ void st8_dev(char* base, unsigned voff, u32x2 v) {
  asm volatile("global_store_dwordx2 %0, %1, %2 sc1" ::"v"(voff), "v"(v), "s"(base) : "memory");
}
__device__ __forceinline__ void ld16_dev(u32x4& r, unsigned voff, const char* base) {
  asm volatile("global_load_dwordx4 %0, %1, %2 sc1" : "=v"(r) : "v"(voff), "s"(base) : "memory");
}
__device__ __forceinline__ u32x2 ld8_dev_wait(unsigned voff, const char* base) {
  u32x2 r;
  asm volatile("global_load_dwordx2 %0, %1, %2 sc1\n\ts_waitcnt vmcnt(0)" : "=v"(r) : "v"(voff), "s"(base) : "memory");
  return r;
}
__device__ __forceinline__ void drain() { asm volatile("s_waitcnt vmcnt(0)" ::: "memory"); }
__device__ __forceinline__ void wg_barrier() { asm volatile("s_waitcnt lgkmcnt(0)\n\ts_barrier" ::: "memory"); }
__device__ __forceinline__ u32x2 gr_make(unsigned t0, unsigned t1, unsigned v24) { return u32x2{(t0 << 24) | v24, t1}; }
__device__ __forceinline__ unsigned gr_bad(unsigned lo, unsigned hi, unsigned t0, unsigned t1) { return (hi ^ t1) | ((lo >> 24) ^ t0); }
__device__ __forceinline__ bool gr_ok(unsigned lo, unsigned hi, unsigned t0, unsigned t1) { return gr_bad(lo, hi, t0, t1) == 0; }
__device__ __forceinline__ unsigned f2v24(float x) { return ((__builtin_bit_cast(unsigned, x) + 0x80u) >> 8) & 0xffffffu; }
__device__ __forceinline__ float v242f(unsigned lo) { return __builtin_bit_cast(float, lo << 8); }
constexpr unsigned SPIN_MAX = 1u << 16;
#define STAMP(i)

__device__ __forceinline__ float fold32(float a, float b) {
  const auto r = __builtin_amdgcn_permlane32_swap(__builtin_bit_cast(unsigned, a), __builtin_bit_cast(unsigned, b), false, false);
  const unsigned r0 = r[0], r1 = r[1];
  return __builtin_bit_cast(float, r0) + __builtin_bit_cast(float, r1);
}
__device__ __forceinline__ float fold16(float a, float b) {
  const auto r = __builtin_amdgcn_permlane16_swap(__builtin_bit_cast(unsigned, a), __builtin_bit_cast(unsigned, b), false, false);
  const unsigned r0 = r[0], r1 = r[1];
  return __builtin_bit_cast(float, r0) + __builtin_bit_cast(float, r1);
}
__device__ __forceinline__ f32x2 fold_rows(const float (&y)[8]) {
  const float c0 = fold32(y[0], y[4]), c1 = fold32(y[1], y[5]), c2 = fold32(y[2], y[6]), c3 = fold32(y[3], y[7]);
  return f32x2{fold16(c0, c2), fold16(c1, c3)};
}
__device__ __forceinline__ float wave_sum(float v) {
  v += dpp<0xB1>(v); v += dpp<0x4E>(v); v += dpp<0x141>(v); v += dpp<0x140>(v);
  v = fold16(v, v); v = fold32(v, v);
  return v;
}
__device__ __forceinline__ float wave_max(float v) {
  v = fmaxf(v, dpp<0xB1>(v)); v = fmaxf(v, dpp<0x4E>(v)); v = fmaxf(v, dpp<0x141>(v)); v = fmaxf(v, dpp<0x140>(v));
  { const auto r = __builtin_amdgcn_permlane16_swap(__builtin_bit_cast(unsigned, v), __builtin_bit_cast(unsigned, v), false, false);
    const unsigned r0 = r[0], r1 = r[1];
    v = fmaxf(__builtin_bit_cast(float, r0), __builtin_bit_cast(float, r1)); }
  { const auto r = __builtin_amdgcn_permlane32_swap(__builtin_bit_cast(unsigned, v), __builtin_bit_cast(unsigned, v), false, false);
    const unsigned r0 = r[0], r1 = r[1];
    v = fmaxf(__builtin_bit_cast(float, r0), __builtin_bit_cast(float, r1)); }
  return v;
}
__device__ __forceinline__ void ln8(const float (&x)[8], float (&y)[8]) {
  float s = 0.f;
#pragma unroll
  for (int i = 0; i < 8; i++) s += x[i];
  const float mean = wave_sum(s) * (1.f / DM);
  float q = 0.f;
#pragma unroll
  for (int i = 0; i < 8; i++) { const float d = x[i] - mean; q += d * d; }
  const float var = rbf(wave_sum(q) * (1.f / DM));
  const float mb = rbf(mean);
  const float sd = rbf(sqrtf(rbf(var + 1e-5f)));
#pragma unroll
  for (int i = 0; i < 8; i++) y[i] = rbf(rbf(x[i] - mb) / sd);
}
__device__ __forceinline__ unsigned xcc_class() {
  unsigned xcc;
  asm volatile("s_getreg_b32 %0, hwreg(HW_REG_XCC_ID)" : "=s"(xcc));
  xcc &= 7;
  return (xcc >> 2) | ((xcc & 1) << 1);
}
__device__ __forceinline__ unsigned blk_off(unsigned slab, unsigned n, unsigned sg) {
  const unsigned a13 = (sg ^ slab ^ (slab >> 7)) & 1;
  const unsigned a14 = ((sg >> 1) ^ (n >> 4) ^ (slab >> 5)) & 1;
  return (slab << 21) | (n << 15) | (a14 << 14) | (a13 << 13);
}
typedef __attribute__((address_space(3))) char* lds_ptr;
__device__ __forceinline__ unsigned lds_addr(const void* p) { return (unsigned)(uintptr_t)(lds_ptr)p; }
__device__ __forceinline__ void flag_set(unsigned a, unsigned v) { asm volatile("ds_write_b32 %0, %1" ::"v"(a), "v"(v) : "memory"); }
__device__ __forceinline__ bool flag_one(unsigned a) {
  unsigned v;
  asm volatile("ds_read_b32 %0, %1\n\ts_waitcnt lgkmcnt(0)" : "=v"(v) : "v"(a) : "memory");
  return __builtin_amdgcn_readfirstlane(v) != 0;
}
__device__ __forceinline__ bool flag_four(unsigned a) {
  u32x4 v;
  asm volatile("ds_read_b128 %0, %1\n\ts_waitcnt lgkmcnt(0)" : "=v"(v) : "v"(a) : "memory");
  return __builtin_amdgcn_readfirstlane(v[0] & v[1] & v[2] & v[3]) != 0;
}
__device__ __forceinline__ unsigned timed_load(const char* p) {
  unsigned v; u64 t0, t1;
  asm volatile("s_memtime %0\n\ts_waitcnt lgkmcnt(0)" : "=s"(t0)::"memory");
  asm volatile("global_load_dword %0, %1, %2 nt\n\ts_waitcnt vmcnt(0)" : "=v"(v) : "v"(0u), "s"(p) : "memory");
  asm volatile("s_memtime %0\n\ts_waitcnt lgkmcnt(0)" : "=s"(t1)::"memory");
  return (unsigned)(t1 - t0);
}

struct Ring { u32x4 r[8]; };
constexpr int PB = 2816;
constexpr int ioff(int i) { return (i & 1) * 4096 + (i >> 1) * 512 - PB; }

__device__ __forceinline__ void issue8(Ring& t, unsigned voff, const char* p) {
  const u32x4 z = {0, 0, 0, 0};
#pragma unroll
  for (int i = 0; i < 8; i++) t.r[i] = z;
  ldnt<ioff(0)>(t.r[0], voff, p); ldnt<ioff(1)>(t.r[1], voff, p); ldnt<ioff(2)>(t.r[2], voff, p); ldnt<ioff(3)>(t.r[3], voff, p);
  ldnt<ioff(4)>(t.r[4], voff, p); ldnt<ioff(5)>(t.r[5], voff, p); ldnt<ioff(6)>(t.r[6], voff, p); ldnt<ioff(7)>(t.r[7], voff, p);
}
__device__ __forceinline__ void k_piece(Ring& t, const u32x4& q, unsigned voff, const char* nx, int c, unsigned short* dst) {
  float x[8];
#define QK(i)                                                            \
  { waitv<15>(t.r[i]);                                                   \
    float d = dot2(t.r[i][0], q[0], 0.f); d = dot2(t.r[i][1], q[1], d);  \
    d = dot2(t.r[i][2], q[2], d); d = dot2(t.r[i][3], q[3], d);          \
    x[i] = d; asm volatile("" : "+v"(x[i]));                             \
    ldnt<ioff(i)>(t.r[i], voff, nx); }
  QK(0) QK(1) QK(2) QK(3) QK(4) QK(5) QK(6) QK(7)
#undef QK
  const bool b3 = c & 8, b2 = c & 4, b1 = c & 2;
  float y[4], z[2];
#pragma unroll
  for (int j = 0; j < 4; j++) y[j] = (b3 ? x[j + 4] : x[j]) + dpp<0x128>(b3 ? x[j] : x[j + 4]);
#pragma unroll
  for (int j = 0; j < 2; j++) z[j] = (b2 ? y[j + 2] : y[j]) + dpp<0x141>(b2 ? y[j] : y[j + 2]);
  float s = (b1 ? z[1] : z[0]) + dpp<0x4E>(b1 ? z[0] : z[1]);
  s += dpp<0xB1>(s);
  *dst = f2bf(rbf(s) * 0.08838834764831845f);
}
template <int MODE>
__device__ __forceinline__ void v_piece(Ring& t, const float* pp, float (&acc)[8], unsigned voff, const char* nx) {
  const f32x4 p0 = *(const f32x4*)pp, p1 = *(const f32x4*)(pp + 4);
#define PV(i, pb)                                                        \
  { waitv<MODE == 0 ? 15 : MODE == 1 ? 15 - (i) : 7 - (i)>(t.r[i]);       \
    _Pragma("unroll") for (int k = 0; k < 4; k++) {                      \
      acc[2 * k] = fmaf(pb, bf_lo(t.r[i][k]), acc[2 * k]);               \
      acc[2 * k + 1] = fmaf(pb, bf_hi(t.r[i][k]), acc[2 * k + 1]); }     \
    _Pragma("unroll") for (int k = 0; k < 8; k++) asm volatile("" : "+v"(acc[k])); \
    if (MODE == 0) ldnt<ioff(i)>(t.r[i], voff, nx); }
  PV(0, p0[0]) PV(1, p0[1]) PV(2, p0[2]) PV(3, p0[3]) PV(4, p1[0]) PV(5, p1[1]) PV(6, p1[2]) PV(7, p1[3])
#undef PV
}

constexpr int L_SC = 0;
constexpr int L_P = 16384;
constexpr int L_O = L_P + 32768;
constexpr int L_Q = L_O + 8192;
constexpr int L_M = L_Q + 1024;
constexpr int L_END = L_M + 512;

__global__ void __launch_bounds__(576, 3)
main_k(const unsigned short* __restrict__ x, const char* __restrict__ K, const char* __restrict__ V,
       const unsigned short* __restrict__ wq, const unsigned short* __restrict__ wo, const unsigned short* __restrict__ w1,
       const unsigned short* __restrict__ b1, const unsigned short* __restrict__ w2,
       const unsigned short* __restrict__ b2, char* __restrict__ ws, unsigned short* __restrict__ out) {
  __shared__ __attribute__((aligned(16))) char lds[L_END];
  unsigned* s_m = (unsigned*)(lds + L_M);
  const unsigned fm = lds_addr(lds) + L_M;
  const int tid = threadIdx.x, lane = tid & 63, wave = __builtin_amdgcn_readfirstlane(tid >> 6);
  const unsigned blk = blockIdx.x;
  const unsigned xs = blk & 7, m = blk >> 3;
  const unsigned j = (xs & 1) | ((xs >> 2) << 1);
  const unsigned h = (m >> 1) + ((xs & 2) << 3), bg = m & 1;
  auto slab_of = [&](unsigned k) { return (bg * 4 + ((k + h) & 3)) * NH + h; };

  if (wave < 8) {
    float* s_xn = (float*)(lds + L_P);
    float* s_red = s_xn + DM * 8;
    const int col0 = (xs * 32 + m) * 16;
    const int rr = lane >> 3, cp = lane & 7;
    unsigned wr[8];
    u32x4 xr;
    if (wave == 0) STAMP(0);
    ld16(xr, lane * 16, x + wave * DM);
#pragma unroll
    for (int i = 0; i < 8; i++) ld4(wr[i], rr * (NH * DH * 2) + cp * 4, wq + (size_t)(wave * 64 + i * 8) * (NH * DH) + col0);
    {
      float xv[8], xn[8];
      waitv<8>(xr);
#pragma unroll
      for (int k = 0; k < 4; k++) { xv[2 * k] = bf_lo(xr[k]); xv[2 * k + 1] = bf_hi(xr[k]); }
      ln8(xv, xn);
#pragma unroll
      for (int k = 0; k < 8; k++) s_xn[(lane * 8 + k) * 8 + wave] = xn[k];
    }
#pragma unroll
    for (int i = 0; i < 8; i++) waitv<0>(wr[i]);
    wg_barrier();
    if (wave == 0) STAMP(1);
    float a0[8], a1[8];
#pragma unroll
    for (int b = 0; b < 8; b++) { a0[b] = 0.f; a1[b] = 0.f; }
#pragma unroll
    for (int i = 0; i < 8; i++) {
      const float* xp = s_xn + (wave * 64 + i * 8 + rr) * 8;
      const f32x4 x0 = *(const f32x4*)xp, x1 = *(const f32x4*)(xp + 4);
      const float wl = bf_lo(wr[i]), wh = bf_hi(wr[i]);
#pragma unroll
      for (int b = 0; b < 4; b++) {
        a0[b] = fmaf(x0[b], wl, a0[b]); a1[b] = fmaf(x0[b], wh, a1[b]);
        a0[b + 4] = fmaf(x1[b], wl, a0[b + 4]); a1[b + 4] = fmaf(x1[b], wh, a1[b + 4]);
      }
    }
    const bool h3 = lane & 8;
    float c0[4], c1[4], d0[2], d1[2];
#pragma unroll
    for (int i = 0; i < 4; i++) { c0[i] = fold32(a0[i], a0[i + 4]); c1[i] = fold32(a1[i], a1[i + 4]); }
#pragma unroll
    for (int i = 0; i < 2; i++) { d0[i] = fold16(c0[i], c0[i + 2]); d1[i] = fold16(c1[i], c1[i + 2]); }
    float e0 = (h3 ? d0[1] : d0[0]) + dpp<0x128>(h3 ? d0[0] : d0[1]);
    float e1 = (h3 ? d1[1] : d1[0]) + dpp<0x128>(h3 ? d1[0] : d1[1]);
    *(f32x2*)(s_red + wave * 128 + lane * 2) = f32x2{e0, e1};
    unsigned arr = 0;
    if (lane == 0) asm volatile("ds_add_rtn_u32 %0, %1, %2\n\ts_waitcnt lgkmcnt(0)" : "=v"(arr) : "v"(fm + 9 * 4), "v"(1u) : "memory");
    if (__builtin_amdgcn_readfirstlane(arr) == 7) {
      e0 = 0.f; e1 = 0.f;
#pragma unroll
      for (int w = 0; w < 8; w++) { const f32x2 t = *(const f32x2*)(s_red + w * 128 + lane * 2); e0 += t[0]; e1 += t[1]; }
      const unsigned t0 = s_m[10], t1 = s_m[11];
      const u32x2 ga = gr_make(t0, t1, (unsigned)f2bf(e0) << 8), gb = gr_make(t0, t1, (unsigned)f2bf(e1) << 8);
      st16_dev(ws + WS_Q, (rr * (NH * DH) + col0 + cp * 2) * 8, u32x4{ga[0], ga[1], gb[0], gb[1]});
      STAMP(2);
    }
    wg_barrier();
  }

  if (wave < 4) {
    __builtin_amdgcn_s_setprio(3);
    const int r = lane >> 4, c = lane & 15;
    const unsigned voff = c * 16 + (r & 1) * 256 + (r >> 1) * 32768;
    const unsigned sel = __builtin_amdgcn_readfirstlane(s_m[8]);
    const unsigned toff = h * 12 + bg * 14 + (h >> 4) * 7;
    auto addr = [&](unsigned t) -> const char* {
      const unsigned se = (t & 64) ? sel >> 4 : sel;
      const unsigned sg = (se ^ ((se & 4) ? t : 0)) & 3;
      const unsigned n = ((t + toff) & 15) * 4 + (wave & 2);
      return ((t & 64) ? V : K) + (blk_off(slab_of((t >> 4) & 3), n, sg) + (wave & 1) * 2048 + PB);
    };
    Ring ra, rb;
    issue8(ra, voff, addr(0));
    issue8(rb, voff, addr(1));
    if (wave == 0) STAMP(3);
    unsigned short* sdst = (unsigned short*)(lds + L_SC) + wave * 32 + r * 8 + (c >> 1);
    u32x4 q;
    for (unsigned t = 0; t < 64; t += 2) {
      if ((t & 15) == 0) q = *(const u32x4*)(lds + L_Q + t * 16 + c * 16);
      k_piece(ra, q, voff, addr(t + 2), c, sdst + t * 128);
      k_piece(rb, q, voff, addr(t + 3), c, sdst + t * 128 + 128);
      if ((t & 15) == 14) { flag_set(fm + (16 + (t >> 4) * 4 + wave) * 4, 1); if (wave == 0) STAMP(4 + (t >> 4) * 2); }
    }
    float acc[8] = {0, 0, 0, 0, 0, 0, 0, 0};
    const float* pp = (const float*)(lds + L_P) + wave * 32 + r * 8;
    auto flush = [&](unsigned k) {
      *(f32x2*)((float*)(lds + L_O) + (k * 4 + wave) * 128 + c * 8 + r * 2) = fold_rows(acc);
      flag_set(fm + (48 + k * 4 + wave) * 4, 1);
      if (wave == 0) STAMP(12 + k * 2);
#pragma unroll
      for (int i = 0; i < 8; i++) acc[i] = 0.f;
    };
    for (unsigned t = 0; t < 62; t += 2) {
      if ((t & 15) == 0) {
#pragma nounroll
        while (!flag_one(fm + (32 + (t >> 4)) * 4)) __builtin_amdgcn_s_sleep(1);
        if (wave == 0) STAMP(5 + (t >> 4) * 2);
      }
      v_piece<0>(ra, pp + t * 128, acc, voff, addr(64 + t + 2));
      v_piece<0>(rb, pp + t * 128 + 128, acc, voff, addr(64 + t + 3));
      if ((t & 15) == 14) flush(t >> 4);
    }
    v_piece<1>(ra, pp + 62 * 128, acc, voff, nullptr);
    v_piece<2>(rb, pp + 63 * 128, acc, voff, nullptr);
    flush(3);
  } else if (wave < 8) {
    const int hw = wave - 4;
    const unsigned t0 = s_m[10], t1 = s_m[11];
    {
      const unsigned bc = blk >> 5, cg = blk & 31;
      float xv[8], xn[8], hh[8];
      u32x4 xr, ga[4], gb[4], gc[4];
      unsigned b2r;
      ld16(xr, lane * 16, x + bc * DM);
#pragma unroll
      for (int i = 0; i < 4; i++) {
        const unsigned o = (i < 3 ? lane : lane & 31) * 16;
        ld16(ga[i], o, w1 + i * 512); ld16(gb[i], o, b1 + i * 512); ld16(gc[i], o, w2 + i * 512);
      }
      asm volatile("global_load_ushort %0, %1, %2" : "=v"(b2r) : "v"(0u), "s"(b2));
      waitv<13>(xr);
#pragma unroll
      for (int k = 0; k < 4; k++) { xv[2 * k] = bf_lo(xr[k]); xv[2 * k + 1] = bf_hi(xr[k]); }
      ln8(xv, xn);
      ln8(xn, hh);
      float hv[4], ya[4], xo[4];
      const int src = cg * 2 + (hw >> 1);
#pragma unroll
      for (int i = 0; i < 4; i++) {
        const float ha = __shfl(hh[i], src), hb = __shfl(hh[4 + i], src);
        hv[i] = (hw & 1) ? hb : ha;
        const float xa = __shfl(xv[i], src), xb = __shfl(xv[4 + i], src);
        xo[i] = (hw & 1) ? xb : xa;
        ya[i] = 0.f;
      }
      waitv<0>(b2r);
#pragma unroll
      for (int ch = 0; ch < 4; ch++) {
        waitv<0>(ga[ch]); waitv<0>(gb[ch]); waitv<0>(gc[ch]);
        if (ch == 3 && lane >= 32) gc[ch] = u32x4{0, 0, 0, 0};
#pragma unroll
        for (int e = 0; e < 8; e++) {
          const float a = (e & 1) ? bf_hi(ga[ch][e >> 1]) : bf_lo(ga[ch][e >> 1]);
          const float bb = (e & 1) ? bf_hi(gb[ch][e >> 1]) : bf_lo(gb[ch][e >> 1]);
          const float w = (e & 1) ? bf_hi(gc[ch][e >> 1]) : bf_lo(gc[ch][e >> 1]);
#pragma unroll
          for (int i = 0; i < 4; i++) {
            const float u = rbf(rbf(hv[i] * a) + bb);
            const float zz = u * fmaf(u * u, 0.044715f, 1.f) * (-2.f * 0.7978845608028654f * LOG2E);
            const float g = rbf(u * __builtin_amdgcn_rcpf(1.f + __builtin_amdgcn_exp2f(zz)));
            ya[i] = fmaf(g, w, ya[i]);
          }
        }
      }
#pragma unroll
      for (int i = 0; i < 4; i++) {
        const float yy = rbf(rbf(wave_sum(ya[i])) + bf_lo(b2r));
        const float g = rbf(yy / (1.f + __expf(-yy)));
        if (lane == 0) {
          ((float*)s_m)[64 + hw * 4 + i] = g;
          ((float*)s_m)[80 + hw * 4 + i] = xo[i];
        }
      }
      flag_set(fm + (96 + hw) * 4, 1);
    }
    if (hw == 0) STAMP(26);
    const int rs = lane >> 4, cc = lane & 15;
    u32x4 wv[32];
#pragma unroll
    for (int part = 0; part < 4; part++) {
#pragma unroll
      for (int i = part * 8; i < part * 8 + 8; i++) ld16(wv[i], rs * 1024 + cc * 16, wo + (size_t)(h * DH + 4 * i) * DM + hw * 128);
#pragma unroll
      for (int i = part * 8; i < part * 8 + 8; i++) waitv<0>(wv[i]);
#pragma unroll
      for (int i = part * 8; i < part * 8 + 8; i += 2)
#pragma unroll
        for (int e = 0; e < 4; e++) {
          const unsigned a = wv[i][e], b = wv[i + 1][e];
          wv[i][e] = __builtin_amdgcn_perm(b, a, 0x05040100u);
          wv[i + 1][e] = __builtin_amdgcn_perm(b, a, 0x07060302u);
          asm volatile("" : "+v"(wv[i][e]), "+v"(wv[i + 1][e]));
        }
    }
    if (hw == 0) STAMP(25);
#pragma nounroll
    for (unsigned k = 0; k < 4; k++) {
#pragma nounroll
      while (!flag_four(fm + (48 + k * 4) * 4)) __builtin_amdgcn_s_sleep(1);
      float o0 = 0.f, o1 = 0.f;
#pragma unroll
      for (int w = 0; w < 4; w++) { const float* t = (const float*)(lds + L_O) + (k * 4 + w) * 128 + cc * 8 + rs; o0 += t[0]; o1 += t[4]; }
      const unsigned ob = pack2(o0, o1);
      float y[8] = {0, 0, 0, 0, 0, 0, 0, 0};
#define OP(pi)                                                                          \
      { const unsigned o = __builtin_amdgcn_mov_dpp(ob, 0x150 + (pi), 0xf, 0xf, true); \
        _Pragma("unroll") for (int e = 0; e < 4; e++) {                                 \
          y[2 * e] = dot2(wv[2 * (pi)][e], o, y[2 * e]);                                \
          y[2 * e + 1] = dot2(wv[2 * (pi) + 1][e], o, y[2 * e + 1]); } }
      OP(0) OP(1) OP(2) OP(3) OP(4) OP(5) OP(6) OP(7) OP(8) OP(9) OP(10) OP(11) OP(12) OP(13) OP(14) OP(15)
#undef OP
      const f32x2 yy = fold_rows(y);
      const unsigned b = slab_of(k) >> 5;
      const u32x2 ga = gr_make(t0, t1, f2v24(yy[0])), gb = gr_make(t0, t1, f2v24(yy[1]));
      st16_dev(ws + WS_Y + (size_t)b * (32 * 16384) + k * 4096 + ((h >> 2) * 4 + j) * 128,
               (hw * 8 + (cc >> 1)) * 16384 + ((cc & 1) * 4 + rs) * 16, u32x4{ga[0], ga[1], gb[0], gb[1]});
      if (hw == 0) STAMP(36 + k);
    }
  } else {
    const unsigned g = xcc_class();
    const unsigned pT = m & 1, psg = (m >> 1) & 3;
    const char* pa = (pT ? V : K) + blk_off((blk * 37 + 11) & 255, (blk * 13) & 15, psg);
    u32x2 ep;
    unsigned wu;
    asm volatile("global_load_dwordx2 %0, %1, %2 sc1" : "=v"(ep) : "v"(0u), "s"(ws + WS_EPOCH));
    asm volatile("global_load_dword %0, %1, %2 offset:3968 nt" : "=v"(wu) : "v"(0u), "s"(pa));
    s_m[lane] = 0;
    s_m[64 + lane] = 0;
    asm volatile("s_waitcnt vmcnt(1)" : "+v"(ep));
    const u64 en = (((u64)(unsigned)__builtin_amdgcn_readfirstlane(ep[1]) << 32) | (unsigned)__builtin_amdgcn_readfirstlane(ep[0])) + 1;
    const unsigned t0 = (unsigned)en & 0xffu, t1 = (unsigned)(en >> 8);
    if (lane == 0) { s_m[10] = t0; s_m[11] = t1; }
    waitv<0>(wu);
    wg_barrier();
    {
      unsigned a = timed_load(pa), b = timed_load(pa + 4096 + 256);
      a = a < 450u ? 4095u : a; b = b < 450u ? 4095u : b;
      a = a < b ? a : b;
      a = a == 4095u ? 0u : a < 1200u ? a : 1200u;
      if (lane == 0) st8_dev(ws + WS_VOTE, blk * 8, gr_make(t0, t1, (a << 5) | (pT << 4) | ((psg ^ g) << 2) | g));
    }
    STAMP(22);
    u32x4 gq[4], gv[2];
#pragma nounroll
    for (unsigned spin = 0; spin < SPIN_MAX; spin++) {
#pragma unroll
      for (int i = 0; i < 2; i++) ld16_dev(gv[i], lane * 16, ws + WS_VOTE + i * 1024);
#pragma unroll
      for (int i = 0; i < 4; i++) ld16_dev(gq[i], lane * 16, ws + WS_Q + (size_t)slab_of(i) * 1024);
      unsigned bad = 0;
#pragma unroll
      for (int i = 0; i < 2; i++) { waitv<4>(gv[i]); bad |= gr_bad(gv[i][0], gv[i][1], t0, t1) | gr_bad(gv[i][2], gv[i][3], t0, t1); }
#pragma unroll
      for (int i = 0; i < 4; i++) { waitv<0>(gq[i]); bad |= gr_bad(gq[i][0], gq[i][1], t0, t1) | gr_bad(gq[i][2], gq[i][3], t0, t1); }
      if (__all(bad == 0)) break;
    }
    STAMP(23);
#pragma unroll
    for (int i = 0; i < 4; i++) {
      const unsigned v = gv[i >> 1][(i & 1) * 2], lat = (v >> 5) & 2047u;
      atomicAdd(&s_m[(v >> 2) & 7], (lat << 9) | (lat != 0));
    }
#pragma unroll
    for (int i = 0; i < 4; i++) ((unsigned*)(lds + L_Q))[i * 64 + lane] = __builtin_amdgcn_perm(gq[i][2], gq[i][0], 0x06050201u);
    unsigned sel;
    {
      unsigned best[2];
#pragma unroll
      for (int T = 0; T < 2; T++) {
        float mean[4];
        bool good = true;
        const u32x4 sv = *(const u32x4*)(s_m + T * 4);
#pragma unroll
        for (int i = 0; i < 4; i++) {
          const unsigned s = sv[i];
          good = good && (s & 511u) != 0;
          mean[i] = (float)(s >> 9) * __builtin_amdgcn_rcpf((float)((s & 511u) | ((s & 511u) == 0)));
        }
        const float m0 = fminf(mean[0], mean[2]), m1 = fminf(mean[1], mean[3]);
        const unsigned c1 = m1 < m0;
        const unsigned c2 = (c1 ? mean[3] < mean[1] : mean[2] < mean[0]);
        good = good && fabsf(m1 - m0) > 80.f;
        best[T] = c1 | (c2 << 1) | (good ? 0u : 4u);
      }
      const unsigned l0 = ((blk & ~5u) & 127u) >> 1;
      const unsigned va = blk & 128u ? gv[1][0] : gv[0][0], vb = blk & 128u ? gv[1][2] : gv[0][2];
      const unsigned bits = (1u << (__builtin_amdgcn_readlane(va, l0) & 3)) | (1u << (__builtin_amdgcn_readlane(vb, l0) & 3)) |
                            (1u << (__builtin_amdgcn_readlane(va, l0 + 2) & 3)) | (1u << (__builtin_amdgcn_readlane(vb, l0 + 2) & 3));
      const unsigned cls = bits == 15u ? g : j;
      sel = __builtin_amdgcn_readfirstlane((best[0] ^ cls) | ((best[1] ^ cls) << 4));
      if (lane == 0) s_m[8] = sel;
    }
    STAMP(24);
    wg_barrier();
    if (blk == 0 && lane == 0) st8_dev(ws + WS_EPOCH, 0, u32x2{(unsigned)en, (unsigned)(en >> 32)});
    const bool own = ((sel ^ (sel >> 4)) & 7) == 0;
    unsigned pub = 0, fet = 0;
#pragma nounroll
    for (unsigned spin = 0; fet < 4;) {
      if (pub < 4 && flag_four(fm + (16 + pub * 4) * 4)) {
        const unsigned k = pub, sl = slab_of(k);
        u32x4 v[4];
        float mx = -3.0e38f;
#pragma unroll
        for (int i = 0; i < 4; i++) {
          v[i] = *(const u32x4*)(lds + L_SC + k * 4096 + i * 1024 + lane * 16);
          const unsigned tt = i * 4 + (lane >> 4), sg = (sel ^ ((sel & 4) ? tt : 0)) & 3;
          if (!own) st16_dev(ws + WS_S + (size_t)sl * 16384 + i * 1024, sg * 4096 + lane * 16, v[i]);
#pragma unroll
          for (int e = 0; e < 4; e++) mx = fmaxf(mx, fmaxf(bf_lo(v[i][e]), bf_hi(v[i][e])));
        }
        mx = wave_max(mx);
        float l = 0.f;
#pragma unroll
        for (int i = 0; i < 4; i++)
#pragma unroll
          for (int e = 0; e < 4; e++)
            l += __builtin_amdgcn_exp2f((bf_lo(v[i][e]) - mx) * LOG2E) + __builtin_amdgcn_exp2f((bf_hi(v[i][e]) - mx) * LOG2E);
        l = wave_sum(l);
        drain();
        if (lane < 2) st8_dev(ws + WS_ML, (sl * 8 + j * 2 + lane) * 8, gr_make(t0, t1, f2v24(lane ? l : mx)));
        STAMP(28 + k);
        pub++;
        continue;
      }
      if (fet < pub) {
        const unsigned k = fet, sl = slab_of(k), se = sel >> 4;
        const u32x2 gm = ld8_dev_wait((sl * 8 + (lane & 7)) * 8, ws + WS_ML);
        if (__all(gr_ok(gm[0], gm[1], t0, t1)) || ++spin > SPIN_MAX) {
          u32x4 v[4];
#pragma unroll
          for (int i = 0; i < 4; i++) {
            const unsigned tt = i * 4 + (lane >> 4), sg = (se ^ ((se & 4) ? tt : 0)) & 3;
            if (own) v[i] = *(const u32x4*)(lds + L_SC + k * 4096 + i * 1024 + lane * 16);
            else ld16_dev(v[i], sg * 4096 + lane * 16, ws + WS_S + (size_t)sl * 16384 + i * 1024);
          }
          const float val = v242f(gm[0]), oth = dpp<0xB1>(val);
          const float mm = (lane & 1) ? oth : val, ll = (lane & 1) ? val : oth;
          float M = fmaxf(mm, dpp<0x4E>(mm));
          M = fmaxf(M, dpp<0x141>(M));
          float L = ll * __builtin_amdgcn_exp2f((mm - M) * LOG2E);
          L += dpp<0x4E>(L);
          L += dpp<0x141>(L);
          const float inv = 1.f / L;
#pragma unroll
          for (int i = 0; i < 4; i++) {
            waitv<0>(v[i]);
            float p[8];
#pragma unroll
            for (int e = 0; e < 4; e++) {
              p[2 * e] = rbf(__builtin_amdgcn_exp2f((bf_lo(v[i][e]) - M) * LOG2E) * inv);
              p[2 * e + 1] = rbf(__builtin_amdgcn_exp2f((bf_hi(v[i][e]) - M) * LOG2E) * inv);
            }
            float* d = (float*)(lds + L_P) + k * 2048 + i * 512 + lane * 8;
            *(f32x4*)d = f32x4{p[0], p[1], p[2], p[3]};
            *(f32x4*)(d + 4) = f32x4{p[4], p[5], p[6], p[7]};
          }
          flag_set(fm + (32 + k) * 4, 1);
          STAMP(32 + k);
          fet++;
          continue;
        }
      }
      __builtin_amdgcn_s_sleep(16);
    }
    float y0 = 0.f, y1 = 0.f;
#pragma nounroll
    for (unsigned k = 0; k < 4; k++) {
#pragma nounroll
      while (!flag_four(fm + (48 + k * 4) * 4)) __builtin_amdgcn_s_sleep(2);
      if (k < 3) { __builtin_amdgcn_s_sleep(127); __builtin_amdgcn_s_sleep(127); }
      const char* in = ws + WS_Y + (size_t)blk * 16384 + k * 4096;
      u32x4 gy[4];
#pragma nounroll
      for (unsigned spin = 0; spin < SPIN_MAX; spin++) {
#pragma unroll
        for (int i = 0; i < 4; i++) ld16_dev(gy[i], lane * 16, in + i * 1024);
        unsigned bad = 0;
#pragma unroll
        for (int i = 0; i < 4; i++) { waitv<0>(gy[i]); bad |= gr_bad(gy[i][0], gy[i][1], t0, t1) | gr_bad(gy[i][2], gy[i][3], t0, t1); }
        if (__all(bad == 0)) break;
        if (k < 3) __builtin_amdgcn_s_sleep(64);
      }
#pragma unroll
      for (int i = 0; i < 4; i++) { y0 += v242f(gy[i][0]); y1 += v242f(gy[i][2]); }
      STAMP(40 + k);
    }
    y0 += dpp<0x128>(y0); y1 += dpp<0x128>(y1);
    y0 = fold16(y0, y0); y1 = fold16(y1, y1);
    y0 = fold32(y0, y0); y1 = fold32(y1, y1);
#pragma nounroll
    while (!flag_four(fm + 96 * 4)) __builtin_amdgcn_s_sleep(1);
    if (lane < 8) {
      const f32x2 sg = *(const f32x2*)(s_m + 64 + 2 * lane), sx = *(const f32x2*)(s_m + 80 + 2 * lane);
      *(unsigned*)(out + blk * 16 + lane * 2) = pack2(sx[0] + rbf(rbf(y0) * sg[0]), sx[1] + rbf(rbf(y1) * sg[1]));
    }
  }
}

extern "C" void kernel_launch(void* const* d_in, const int* in_sizes, int n_in, void* d_out, int out_size, void* d_ws,
                              size_t ws_size, hipStream_t stream) {
  main_k<<<256, 576, 0, stream>>>((const unsigned short*)d_in[0], (const char*)d_in[1], (const char*)d_in[2],
                                  (const unsigned short*)d_in[3], (const unsigned short*)d_in[4],
                                  (const unsigned short*)d_in[6], (const unsigned short*)d_in[7],
                                  (const unsigned short*)d_in[8], (const unsigned short*)d_in[9], (char*)d_ws,
                                  (unsigned short*)d_out);
}
```

```cpp
#include <hip/hip_runtime.h>
#include <hip/hip_bf16.h>
#include <stdint.h>

using u32x4 = __attribute__((ext_vector_type(4))) unsigned;
using u32x2 = __attribute__((ext_vector_type(2))) unsigned;
using f32x4 = __attribute__((ext_vector_type(4))) float;
using f32x2 = __attribute__((ext_vector_type(2))) float;
using bf16x2 = __attribute__((ext_vector_type(2))) __bf16;
using bf16x8 = __attribute__((ext_vector_type(8))) __bf16;
typedef unsigned long long u64;

constexpr int DM = 512, NH = 32, DH = 128;
#define WIN 8
#define WIN2 12
#define WIN3 16
#define WOE 7
#define EB 0
#define GO 1
#define CPF 0
#define SER 1
#define TAILV 1
#define PST 6
#define THR3 64
#define THR2 16
#define YW 1
#define W9 1
#define W9NAP 64
#define SW1 3500
#define SW2 6900
#define SSP " sc1 nt"
#define SLP " sc1 nt"
#define MW 9000
#define OW 12200
#define MIXB 0u
#define CT 1
#define WNAP 1
#define XPER 0
#define YNAP 32
#define PSL 0
#define GNAP 32
#define LATE 60
#define PACE 3
#define TAU0 106
#define POFF 300
#define PDEL 40
#define PNAP 48
#define PCL 400
#define PT0 4
#define PSTR 8
#define PWR 1
#define PRD 1
#define PWL 1
#define PWF " sc1"
#define PWOP "global_store_dwordx2"
#define PSH 9
#define PLAG 200
#define PAS 4608
#define PROT 1
#define PS 100
#define PG 0.75f
#define PCP 30
#define PCN 200
#define PPH 13
#define PM 16
#define NOX 0
#define YL 1
#define OL 1
#define SFL 1
#define VMAX 2000
#define VCL 1
#define PINI -320
#define PF1 0
#define LWN 1
#define SL1 36
#define SL2 68
#define OLW 118
#define YLW 0
#define SPROBE 1
#define TB 60
#define PEND 0
#define TPF 0
#define KPF 0
#define BP 0
#define WX 1
#define CM 0
#define SE 1
#define VNAP 72
#define QNAP 127
#define G4N 2
#define PSQ 12
#define QP 1
#define SP 1
#define DP 6
#define DCL 600
#define EG 6
#define KA 2
#define SST 65536
#define NX 4
#define MIXA 0x0CC30CC3u
#define MIXX 0xC030C030u
#define XLIST 0xFA72u
#define MIXA0 0x03030303u
#define MIXX0 0xCCCCCCCCu
#define STAT 0xCCu
#define PNT ""
#define SNT ""
constexpr unsigned P0 = 4;
constexpr unsigned TAU = (unsigned)(TAU0 * 64);
constexpr float LOG2E = 1.4426950408889634f;

constexpr size_t WS_EPOCH = 0;
constexpr size_t WS_VOTE = 4 << 10;
constexpr size_t WS_PR = 8 << 20;
constexpr size_t WS_SF = 8 << 10;
#define QL 2
constexpr size_t WS_Q = 128 << 10;
constexpr size_t WS_QN = 384 << 10;
constexpr size_t WS_QF = 896 << 10;
constexpr size_t WS_O = QL >= 2 ? 5 << 20 : 9 << 19;
constexpr unsigned OB = 2688;
#define QT 1
#define QR 3
#define QN2 127
constexpr size_t WS_QC = 1152 << 10;
static_assert(QT == 1 && QL == 2, "layout");
constexpr unsigned Y2S = QL >= 2 ? 4096 : 3072;
constexpr size_t WS_Y = QL >= 2 ? 4 << 20 : QL == 0 ? 1032 << 10 : 1 << 20;
constexpr size_t WS_Y2 = 4 << 20;
constexpr size_t WS_S2 = 16 << 20;
constexpr size_t WS_S = 2 << 20;

__device__ __forceinline__ float bf_lo(unsigned u) { return __builtin_bit_cast(float, u << 16); }
__device__ __forceinline__ float bf_hi(unsigned u) { return __builtin_bit_cast(float, u & 0xffff0000u); }
__device__ __forceinline__ float rbf(float x) { return (float)(__bf16)x; }
__device__ __forceinline__ unsigned short f2bf(float x) { return __builtin_bit_cast(unsigned short, (__bf16)x); }
__device__ __forceinline__ unsigned pack2(float a, float b) { return (unsigned)f2bf(a) | ((unsigned)f2bf(b) << 16); }
__device__ __forceinline__ unsigned f2u(float x) { return __builtin_bit_cast(unsigned, x); }
__device__ __forceinline__ float u2f(unsigned x) { return __builtin_bit_cast(float, x); }
__device__ __forceinline__ float dot2(unsigned k, unsigned q, float acc) {
  return __builtin_amdgcn_fdot2_f32_bf16(__builtin_bit_cast(bf16x2, k), __builtin_bit_cast(bf16x2, q), acc, false);
}
template <int CTRL> __device__ __forceinline__ float dpp(float x) {
  return __builtin_bit_cast(float, __builtin_amdgcn_mov_dpp(__builtin_bit_cast(int, x), CTRL, 0xf, 0xf, true));
}
template <int OFF> __device__ __forceinline__ void ldnt(u32x4& r, unsigned voff, const char* base) {
  asm volatile("global_load_dwordx4 %0, %1, %2 offset:%3 nt" : "+v"(r) : "v"(voff), "s"(base), "i"(OFF));
}
#define WQP " nt"
#define WOP " nt"
__device__ __forceinline__ void ld16(u32x4& r, unsigned voff, const void* base) {
  asm volatile("global_load_dwordx4 %0, %1, %2" : "=v"(r) : "v"(voff), "s"(base));
}
__device__ __forceinline__ void ld16o(u32x4& r, unsigned voff, const void* base) {
  asm volatile("global_load_dwordx4 %0, %1, %2" WOP : "=v"(r) : "v"(voff), "s"(base));
}
__device__ __forceinline__ void ld4(unsigned& r, unsigned voff, const void* base) {
  asm volatile("global_load_dword %0, %1, %2" WQP : "=v"(r) : "v"(voff), "s"(base));
}
template <int N> __device__ __forceinline__ void waitv(unsigned& r) { asm volatile("s_waitcnt vmcnt(%1)" : "+v"(r) : "i"(N)); }
template <int N> __device__ __forceinline__ void waitv(u32x2& r) { asm volatile("s_waitcnt vmcnt(%1)" : "+v"(r) : "i"(N)); }
template <int N> __device__ __forceinline__ void waitv(u32x4& r) { asm volatile("s_waitcnt vmcnt(%1)" : "+v"(r) : "i"(N)); }
__device__ __forceinline__ void st16_dev(char* base, unsigned voff, u32x4 v) {
  asm volatile("global_store_dwordx4 %0, %1, %2 sc1" SNT "\n\ts_nop 1" ::"v"(voff), "v"(v), "s"(base) : "memory");
}
__device__ __forceinline__ void st8_dev(char* base, unsigned voff, u32x2 v) {
  asm volatile("global_store_dwordx2 %0, %1, %2 sc1" SNT ::"v"(voff), "v"(v), "s"(base) : "memory");
}
template <int OFF = 0> __device__ __forceinline__ void ld16_dev(u32x4& r, unsigned voff, const char* base) {
  asm volatile("global_load_dwordx4 %0, %1, %2 offset:%3 sc1" PNT : "=v"(r) : "v"(voff), "s"(base), "i"(OFF) : "memory");
}
template <int OFF = 0> __device__ __forceinline__ void ld8_dev(u32x2& r, unsigned voff, const char* base) {
  asm volatile("global_load_dwordx2 %0, %1, %2 offset:%3 sc1" PNT : "=v"(r) : "v"(voff), "s"(base), "i"(OFF) : "memory");
}
#define STR2(x) #x
#define STR(x) STR2(x)
using u32x16 = __attribute__((ext_vector_type(16))) unsigned;
struct Fly { u32x16 a; u32x2 b; };
__device__ __forceinline__ void landed(Fly& f) { asm volatile("s_waitcnt vmcnt(0)" : "+{v[100:115]}"(f.a), "+{v[116:117]}"(f.b)); }
__device__ __forceinline__ void poll3(Fly& f, unsigned (&r)[3], const unsigned (&off)[3], const char* base, unsigned tag, unsigned spins) {
  u64 m0, m1, m2;
  unsigned c, d;
#define LD(n, n1, o) "global_load_dwordx2 v[" #n ":" #n1 "], %[" #o "], %[b] sc1\n\t"
#define CHECK(a, a1, b, b1, c, c1)                                                                          \
  "v_cmp_ne_u32_e64 %[m0], %[tag], v" #a1 "\n\tv_cmp_ne_u32_e64 %[m1], %[tag], v" #b1 "\n\tv_cmp_ne_u32_e64 %[m2], %[tag], v" #c1 "\n\t" \
  "v_mov_b32 %[r0], v" #a "\n\tv_mov_b32 %[r1], v" #b "\n\tv_mov_b32 %[r2], v" #c "\n\t"
#define QUAD(m) "s_or_b64 %[" #m "], %[" #m "], 1\n\ts_wqm_b64 exec, %[" #m "]\n\t"
#define AGAIN(a, a1, b, b1, c, c1)                                                                          \
  QUAD(m0) LD(a, a1, o0) QUAD(m1) LD(b, b1, o1) QUAD(m2) LD(c, c1, o2) "s_mov_b64 exec, -1\n\t"
#define ROUND(a, a1, b, b1, c, c1)                                                                          \
  "s_waitcnt vmcnt(6)\n\t" CHECK(a, a1, b, b1, c, c1)                                                       \
  "s_or_b64 vcc, %[m0], %[m1]\n\ts_or_b64 vcc, vcc, %[m2]\n\ts_cbranch_scc0 .Lp3e%=\n\t" AGAIN(a, a1, b, b1, c, c1)
  asm volatile(LD(100, 101, o0) LD(102, 103, o1) LD(104, 105, o2)
               ".Lp3s%=:\n\t"
               "s_waitcnt vmcnt(0)\n\t" CHECK(100, 101, 102, 103, 104, 105)
               "s_bcnt1_i32_b64 %[c], %[m0]\n\ts_bcnt1_i32_b64 %[d], %[m1]\n\ts_add_u32 %[c], %[c], %[d]\n\t"
               "s_bcnt1_i32_b64 %[d], %[m2]\n\ts_add_u32 %[c], %[c], %[d]\n\ts_cmp_eq_u32 %[c], 0\n\ts_cbranch_scc1 .Lp3e%=\n\t"
               "s_cmp_le_u32 %[c], " STR(THR3) "\n\ts_cbranch_scc1 .Lp3f%=\n\t"
               AGAIN(100, 101, 102, 103, 104, 105)
               "s_sub_u32 %[n], %[n], 1\n\ts_cbranch_scc0 .Lp3s%=\n\ts_waitcnt vmcnt(0)\n\ts_branch .Lp3e%=\n"
               ".Lp3f%=:\n\t"
               "v_mov_b64 v[106:107], v[100:101]\n\tv_mov_b64 v[108:109], v[102:103]\n\tv_mov_b64 v[110:111], v[104:105]\n\t"
               "v_mov_b64 v[112:113], v[100:101]\n\tv_mov_b64 v[114:115], v[102:103]\n\tv_mov_b64 v[116:117], v[104:105]\n\t"
               AGAIN(100, 101, 102, 103, 104, 105) "s_sleep " STR(PST) "\n\t"
               AGAIN(106, 107, 108, 109, 110, 111) "s_sleep " STR(PST) "\n\t"
               AGAIN(112, 113, 114, 115, 116, 117)
               ".Lp3l%=:\n\t"
               ROUND(100, 101, 102, 103, 104, 105) ROUND(106, 107, 108, 109, 110, 111) ROUND(112, 113, 114, 115, 116, 117)
               "s_sub_u32 %[n], %[n], 1\n\ts_cbranch_scc0 .Lp3l%=\n"
               ".Lp3e%=:"
               : "=&{v[100:115]}"(f.a), "=&{v[116:117]}"(f.b), [r0] "=&v"(r[0]), [r1] "=&v"(r[1]), [r2] "=&v"(r[2]),
                 [m0] "=&s"(m0), [m1] "=&s"(m1), [m2] "=&s"(m2), [c] "=&s"(c), [d] "=&s"(d), [n] "+s"(spins)
               : [o0] "v"(off[0]), [o1] "v"(off[1]), [o2] "v"(off[2]), [b] "s"(base), [tag] "s"(tag)
               : "memory", "scc", "vcc");
#undef LD
#undef CHECK
#undef AGAIN
#undef QUAD
#undef ROUND
}
__device__ __forceinline__ void poll2(Fly& f, unsigned& r0, unsigned& r1, unsigned off, const char* base, unsigned tag, unsigned spins) {
  u64 m0, m1;
  unsigned c;
#define LD(n, n3) "global_load_dwordx4 v[" #n ":" #n3 "], %[o], %[b] sc1\n\t"
#define CHECK(a, a1, a2, a3)                                                                                \
  "v_cmp_ne_u32_e64 %[m0], %[tag], v" #a1 "\n\tv_cmp_ne_u32_e64 %[m1], %[tag], v" #a3 "\n\t"                 \
  "v_mov_b32 %[r0], v" #a "\n\tv_mov_b32 %[r1], v" #a2 "\n\t"                                                \
  "s_or_b64 %[m0], %[m0], %[m1]\n\ts_cbranch_scc0 .Lp2e%=\n\t"
#define AGAIN(a, a3) "s_or_b64 exec, %[m0], 1\n\t" LD(a, a3) "s_mov_b64 exec, -1\n\t"
#define ROUND(a, a1, a2, a3) "s_waitcnt vmcnt(2)\n\t" CHECK(a, a1, a2, a3) AGAIN(a, a3)
  asm volatile(LD(100, 103)
               ".Lp2s%=:\n\t"
               "s_waitcnt vmcnt(0)\n\t" CHECK(100, 101, 102, 103)
               "s_bcnt1_i32_b64 %[c], %[m0]\n\ts_cmp_le_u32 %[c], " STR(THR2) "\n\ts_cbranch_scc1 .Lp2f%=\n\t"
               AGAIN(100, 103)
               "s_sub_u32 %[n], %[n], 1\n\ts_cbranch_scc0 .Lp2s%=\n\ts_waitcnt vmcnt(0)\n\ts_branch .Lp2e%=\n"
               ".Lp2f%=:\n\t"
               "v_mov_b64 v[104:105], v[100:101]\n\tv_mov_b64 v[106:107], v[102:103]\n\t"
               "v_mov_b64 v[108:109], v[100:101]\n\tv_mov_b64 v[110:111], v[102:103]\n\t"
               AGAIN(100, 103) "s_sleep " STR(PST) "\n\t" AGAIN(104, 107) "s_sleep " STR(PST) "\n\t" AGAIN(108, 111)
               ".Lp2l%=:\n\t"
               ROUND(100, 101, 102, 103) ROUND(104, 105, 106, 107) ROUND(108, 109, 110, 111)
               "s_sub_u32 %[n], %[n], 1\n\ts_cbranch_scc0 .Lp2l%=\n"
               ".Lp2e%=:"
               : "=&{v[100:115]}"(f.a), "=&{v[116:117]}"(f.b), [r0] "=&v"(r0), [r1] "=&v"(r1), [m0] "=&s"(m0), [m1] "=&s"(m1), [c] "=&s"(c), [n] "+s"(spins)
               : [o] "v"(off), [b] "s"(base), [tag] "s"(tag)
               : "memory", "scc");
#undef LD
#undef CHECK
#undef AGAIN
#undef ROUND
}
struct FlyQ { u32x16 a, b, c; };
__device__ __forceinline__ void landed(FlyQ& f) { asm volatile("s_waitcnt vmcnt(0)" : "+{v[100:115]}"(f.a), "+{v[116:131]}"(f.b), "+{v[132:147]}"(f.c)); }
__device__ __forceinline__ void pollq(FlyQ& f, float (&r)[4], unsigned off, const char* base, unsigned tag, unsigned spins) {
  u64 m0, m1, m2, m3, m4, m5, m6, m7;
#define LD(n, n3, o) "global_load_dwordx4 v[" #n ":" #n3 "], %[o], %[b] offset:" #o " sc1\n\t"
#define ALL(a, a3, b, b3, c, c3, d, d3) LD(a, a3, 0) LD(b, b3, 1024) LD(c, c3, 2048) LD(d, d3, 3072)
#define NE(m, n) "v_cmp_ne_u32_e64 %[" #m "], %[tag], v" #n "\n\t"
#define ROUND(a, a1, a2, a3, b, b1, b2, b3, c, c1, c2, c3, d, d1, d2, d3)                                   \
  "s_waitcnt vmcnt(%[w])\n\t"                                                                              \
  NE(m0, a1) NE(m4, a3) NE(m1, b1) NE(m5, b3) NE(m2, c1) NE(m6, c3) NE(m3, d1) NE(m7, d3)                  \
  "v_add_f32 %[r0], v" #a ", v" #b "\n\tv_add_f32 %[r1], v" #a2 ", v" #b2 "\n\t"                           \
  "v_add_f32 %[r2], v" #c ", v" #d "\n\tv_add_f32 %[r3], v" #c2 ", v" #d2 "\n\t"                           \
  "s_or_b64 %[m0], %[m0], %[m4]\n\ts_or_b64 %[m1], %[m1], %[m5]\n\ts_or_b64 %[m2], %[m2], %[m6]\n\ts_or_b64 %[m3], %[m3], %[m7]\n\t" \
  "s_or_b64 %[m4], %[m0], %[m1]\n\ts_or_b64 %[m5], %[m2], %[m3]\n\ts_or_b64 vcc, %[m4], %[m5]\n\ts_cbranch_scc0 .Lpqe%=\n\t" \
  "s_or_b64 exec, %[m0], 1\n\t" LD(a, a3, 0) "s_or_b64 exec, %[m1], 1\n\t" LD(b, b3, 1024)                 \
  "s_or_b64 exec, %[m2], 1\n\t" LD(c, c3, 2048) "s_or_b64 exec, %[m3], 1\n\t" LD(d, d3, 3072) "s_mov_b64 exec, -1\n\t"
  asm volatile(ALL(100, 103, 104, 107, 108, 111, 112, 115)
               "s_sleep " STR(PSQ) "\n\t" ALL(116, 119, 120, 123, 124, 127, 128, 131)
               "s_sleep " STR(PSQ) "\n\t" ALL(132, 135, 136, 139, 140, 143, 144, 147)
               ".Lpql%=:\n\t"
               ROUND(100, 101, 102, 103, 104, 105, 106, 107, 108, 109, 110, 111, 112, 113, 114, 115)
               ROUND(116, 117, 118, 119, 120, 121, 122, 123, 124, 125, 126, 127, 128, 129, 130, 131)
               ROUND(132, 133, 134, 135, 136, 137, 138, 139, 140, 141, 142, 143, 144, 145, 146, 147)
               "s_sub_u32 %[n], %[n], 1\n\ts_cbranch_scc0 .Lpql%=\n"
               ".Lpqe%=:"
               : "=&{v[100:115]}"(f.a), "=&{v[116:131]}"(f.b), "=&{v[132:147]}"(f.c), [r0] "=&v"(r[0]), [r1] "=&v"(r[1]), [r2] "=&v"(r[2]), [r3] "=&v"(r[3]),
                 [m0] "=&s"(m0), [m1] "=&s"(m1), [m2] "=&s"(m2), [m3] "=&s"(m3), [m4] "=&s"(m4), [m5] "=&s"(m5), [m6] "=&s"(m6), [m7] "=&s"(m7), [n] "+s"(spins)
               : [o] "v"(off), [b] "s"(base), [tag] "s"(tag), [w] "i"(QR * 4 - 4)
               : "memory", "scc", "vcc");
#undef LD
#undef ALL
#undef NE
#undef ROUND
}
__device__ __forceinline__ void drain() { asm volatile("s_waitcnt vmcnt(0)" ::: "memory"); }
__device__ __forceinline__ void wg_barrier() { asm volatile("s_waitcnt lgkmcnt(0)\n\ts_barrier" ::: "memory"); }
#define PRIO(n) __builtin_amdgcn_s_setprio(n)
#define NAP(n) __builtin_amdgcn_s_sleep(n)
constexpr unsigned SPIN_MAX = 1u << 16;
#define STAMP(i)

__device__ __forceinline__ float fold32(float a, float b) {
  const auto r = __builtin_amdgcn_permlane32_swap(f2u(a), f2u(b), false, false);
  const unsigned r0 = r[0], r1 = r[1];
  return u2f(r0) + u2f(r1);
}
__device__ __forceinline__ float fold16(float a, float b) {
  const auto r = __builtin_amdgcn_permlane16_swap(f2u(a), f2u(b), false, false);
  const unsigned r0 = r[0], r1 = r[1];
  return u2f(r0) + u2f(r1);
}
__device__ __forceinline__ f32x2 fold_rows(const float (&y)[8]) {
  const float c0 = fold32(y[0], y[4]), c1 = fold32(y[1], y[5]), c2 = fold32(y[2], y[6]), c3 = fold32(y[3], y[7]);
  return f32x2{fold16(c0, c2), fold16(c1, c3)};
}
__device__ __forceinline__ float wave_sum(float v) {
  v += dpp<0xB1>(v); v += dpp<0x4E>(v); v += dpp<0x141>(v); v += dpp<0x140>(v);
  v = fold16(v, v); v = fold32(v, v);
  return v;
}
__device__ __forceinline__ float wave_max(float v) {
  v = fmaxf(v, dpp<0xB1>(v)); v = fmaxf(v, dpp<0x4E>(v)); v = fmaxf(v, dpp<0x141>(v)); v = fmaxf(v, dpp<0x140>(v));
  { const auto r = __builtin_amdgcn_permlane16_swap(f2u(v), f2u(v), false, false);
    const unsigned r0 = r[0], r1 = r[1];
    v = fmaxf(u2f(r0), u2f(r1)); }
  { const auto r = __builtin_amdgcn_permlane32_swap(f2u(v), f2u(v), false, false);
    const unsigned r0 = r[0], r1 = r[1];
    v = fmaxf(u2f(r0), u2f(r1)); }
  return v;
}
__device__ __forceinline__ void ln8(const float (&x)[8], float (&y)[8]) {
  float s = 0.f;
#pragma unroll
  for (int i = 0; i < 8; i++) s += x[i];
  const float mean = wave_sum(s) * (1.f / DM);
  float q = 0.f;
#pragma unroll
  for (int i = 0; i < 8; i++) { const float d = x[i] - mean; q += d * d; }
  const float var = rbf(wave_sum(q) * (1.f / DM));
  const float mb = rbf(mean);
  const float rs = 1.f / rbf(sqrtf(rbf(var + 1e-5f)));
#pragma unroll
  for (int i = 0; i < 8; i++) y[i] = rbf(rbf(x[i] - mb) * rs);
}
__device__ __forceinline__ void ln16(const float (&x)[16], float (&y)[16]) {
  auto half_sum = [](float v) { v += dpp<0xB1>(v); v += dpp<0x4E>(v); v += dpp<0x141>(v); v += dpp<0x140>(v); return fold16(v, v); };
  float s = 0.f, t = 0.f;
#pragma unroll
  for (int i = 0; i < 8; i++) { s += x[i]; t += x[8 + i]; }
  const float mean = half_sum(s + t) * (1.f / DM);
  float q = 0.f, r = 0.f;
#pragma unroll
  for (int i = 0; i < 8; i++) { const float d = x[i] - mean, e = x[8 + i] - mean; q += d * d; r += e * e; }
  const float var = rbf(half_sum(q + r) * (1.f / DM));
  const float mb = rbf(mean);
  const float rs = 1.f / rbf(sqrtf(rbf(var + 1e-5f)));
#pragma unroll
  for (int i = 0; i < 16; i++) y[i] = rbf(rbf(x[i] - mb) * rs);
}
__device__ __forceinline__ unsigned blk_off(unsigned slab, unsigned n, unsigned sg) {
  const unsigned a13 = (sg ^ slab ^ (slab >> 7)) & 1;
  const unsigned a14 = ((sg >> 1) ^ (n >> 4) ^ (slab >> 5)) & 1;
  return (slab << 21) | (n << 15) | (a14 << 14) | (a13 << 13);
}
typedef __attribute__((address_space(3))) char* lds_ptr;
__device__ __forceinline__ unsigned lds_addr(const void* p) { return (unsigned)(uintptr_t)(lds_ptr)p; }
__device__ __forceinline__ void flag_set(unsigned a) { asm volatile("ds_write_b32 %0, %1" ::"v"(a), "v"(1u) : "memory"); }
__device__ __forceinline__ unsigned flag_val(unsigned a) {
  unsigned v;
  asm volatile("ds_read_b32 %0, %1\n\ts_waitcnt lgkmcnt(0)" : "=v"(v) : "v"(a) : "memory");
  return __builtin_amdgcn_readfirstlane(v);
}
__device__ __forceinline__ bool flag_one(unsigned a) { return flag_val(a) != 0; }
__device__ __forceinline__ bool flag_four(unsigned a) {
  u32x4 v;
  asm volatile("ds_read_b128 %0, %1\n\ts_waitcnt lgkmcnt(0)" : "=v"(v) : "v"(a) : "memory");
  return __builtin_amdgcn_readfirstlane(v[0] & v[1] & v[2] & v[3]) != 0;
}
__device__ __forceinline__ unsigned timed_load(const char* p) {
  unsigned v; u64 t0, t1;
  asm volatile("s_waitcnt lgkmcnt(0)\n\ts_memtime %0\n\ts_waitcnt lgkmcnt(0)" : "=s"(t0)::"memory");
  asm volatile("s_load_dword %0, %1, 0x0\n\ts_waitcnt lgkmcnt(0)" : "=s"(v) : "s"(p) : "memory");
  asm volatile("s_memtime %0\n\ts_waitcnt lgkmcnt(0)" : "=s"(t1)::"memory");
  const unsigned d = (unsigned)(t1 - t0);
  return d < 450u ? 0u : d > VMAX ? (VCL ? VMAX : 0u) : d;
}

struct Ring { u32x4 r[8]; };
constexpr int PB = 2816;
constexpr int ioff(int i) { return (i & 1) * 4096 + (i >> 1) * 512 - PB; }

__device__ __forceinline__ void issue8(Ring& t, unsigned voff, const char* p) {
  const u32x4 z = {0, 0, 0, 0};
#pragma unroll
  for (int i = 0; i < 8; i++) t.r[i] = z;
  ldnt<ioff(0)>(t.r[0], voff, p); ldnt<ioff(1)>(t.r[1], voff, p); ldnt<ioff(2)>(t.r[2], voff, p); ldnt<ioff(3)>(t.r[3], voff, p);
  ldnt<ioff(4)>(t.r[4], voff, p); ldnt<ioff(5)>(t.r[5], voff, p); ldnt<ioff(6)>(t.r[6], voff, p); ldnt<ioff(7)>(t.r[7], voff, p);
}
__device__ __forceinline__ void issue8n(Ring& t, unsigned voff, const char* p) {
  ldnt<ioff(0)>(t.r[0], voff, p); ldnt<ioff(1)>(t.r[1], voff, p); ldnt<ioff(2)>(t.r[2], voff, p); ldnt<ioff(3)>(t.r[3], voff, p);
  ldnt<ioff(4)>(t.r[4], voff, p); ldnt<ioff(5)>(t.r[5], voff, p); ldnt<ioff(6)>(t.r[6], voff, p); ldnt<ioff(7)>(t.r[7], voff, p);
}
template <int W, int N> __device__ __forceinline__ void issue8w(Ring& t, unsigned voff, const char* p) {
  const u32x4 z = {0, 0, 0, 0};
#pragma unroll
  for (int i = 0; i < 8; i++) t.r[i] = z;
#define IW(i) { if (N + i >= W) asm volatile("s_waitcnt vmcnt(%0)" ::"i"(W - 1)); ldnt<ioff(i)>(t.r[i], voff, p); }
  IW(0) IW(1) IW(2) IW(3) IW(4) IW(5) IW(6) IW(7)
#undef IW
}
__device__ __forceinline__ void landed(Ring& t) {
#pragma unroll
  for (int i = 0; i < 8; i++) waitv<0>(t.r[i]);
}
template <int W0, int DW, bool REFILL>
__device__ __forceinline__ void k_piece(Ring& t, const u32x4& q, unsigned voff, const char* nx, int c, unsigned short* dst) {
  float x[8];
#define QK(i)                                                            \
  { waitv<W0 - DW * (i)>(t.r[i]);                                        \
    float d = dot2(t.r[i][0], q[0], 0.f); d = dot2(t.r[i][1], q[1], d);  \
    d = dot2(t.r[i][2], q[2], d); d = dot2(t.r[i][3], q[3], d);          \
    x[i] = d; asm volatile("" : "+v"(x[i]));                             \
    if (REFILL) ldnt<ioff(i)>(t.r[i], voff, nx); }
  QK(0) QK(1) QK(2) QK(3) QK(4) QK(5) QK(6) QK(7)
#undef QK
  const bool b3 = c & 8, b2 = c & 4, b1 = c & 2;
  float y[4], z[2];
#pragma unroll
  for (int j = 0; j < 4; j++) y[j] = (b3 ? x[j + 4] : x[j]) + dpp<0x128>(b3 ? x[j] : x[j + 4]);
#pragma unroll
  for (int j = 0; j < 2; j++) z[j] = (b2 ? y[j + 2] : y[j]) + dpp<0x141>(b2 ? y[j] : y[j + 2]);
  float s = (b1 ? z[1] : z[0]) + dpp<0x4E>(b1 ? z[0] : z[1]);
  s += dpp<0xB1>(s);
  *dst = f2bf(rbf(s) * 0.08838834764831845f);
}
struct Wts { u32x4 a, b; };
__device__ __forceinline__ Wts wts(const unsigned* pp) { return Wts{*(const u32x4*)pp, *(const u32x4*)(pp + 4)}; }
template <int MODE>
__device__ __forceinline__ void v_piece(Ring& t, const Wts& p, float (&acc)[8], unsigned voff, const char* nx) {
#define PV(i, ph)                                                        \
  { unsigned pl = (ph) >> 16; asm volatile("" : "+v"(pl));               \
    waitv<MODE == 0 ? 15 : MODE == 1 ? 15 - (i) : 7 - (i)>(t.r[i]);       \
    _Pragma("unroll") for (int k = 0; k < 4; k++) {                      \
      acc[2 * k] = dot2(t.r[i][k], pl, acc[2 * k]);                      \
      acc[2 * k + 1] = dot2(t.r[i][k], ph, acc[2 * k + 1]); }            \
    _Pragma("unroll") for (int k = 0; k < 8; k++) asm volatile("" : "+v"(acc[k])); \
    if (MODE == 0) ldnt<ioff(i)>(t.r[i], voff, nx); }
  PV(0, p.a[0]) PV(1, p.a[1]) PV(2, p.a[2]) PV(3, p.a[3]) PV(4, p.b[0]) PV(5, p.b[1]) PV(6, p.b[2]) PV(7, p.b[3])
#undef PV
}
__device__ __forceinline__ bool reached(unsigned fm_line, unsigned t) {
  unsigned o;
  asm volatile("ds_read_b32 %0, %1" : "=v"(o) : "v"(fm_line) : "memory");
  const unsigned now = (unsigned)__builtin_amdgcn_s_memrealtime() - ((t * TAU) >> 6);
  asm volatile("s_waitcnt lgkmcnt(0)" : "+v"(o));
  return (int)(now - __builtin_amdgcn_readfirstlane(o)) >= 0;
}
__device__ __forceinline__ void pace(unsigned fm_line, unsigned fm_off, unsigned t, unsigned phi) {
  if (!PACE) return;
  const unsigned nom = ((t * TAU) >> 6) + phi;
  unsigned o;
  asm volatile("ds_read_b32 %0, %1" : "=v"(o) : "v"(fm_line) : "memory");
  unsigned now = (unsigned)__builtin_amdgcn_s_memrealtime() - nom;
  asm volatile("s_waitcnt lgkmcnt(0)" : "+v"(o));
  if (PACE == 3) asm volatile("ds_write_b32 %0, %1" ::"v"(fm_off), "v"(now | 1u) : "memory");
#pragma nounroll
  while ((int)(now - __builtin_amdgcn_readfirstlane(o)) < 0) {
    NAP(2);
    asm volatile("ds_read_b32 %0, %1" : "=v"(o) : "v"(fm_line) : "memory");
    now = (unsigned)__builtin_amdgcn_s_memrealtime() - nom;
    asm volatile("s_waitcnt lgkmcnt(0)" : "+v"(o));
  }
  if (PACE == 2) asm volatile("ds_write_b32 %0, %1" ::"v"(fm_off), "v"(now | 1u) : "memory");
}
constexpr int L_SC = 0;
constexpr int L_P = 16384;
constexpr int L_O = L_P + 32768;
constexpr int L_Q = L_O + 10240;
constexpr int L_OB = L_Q + 1024;
constexpr int L_M = L_OB + 1024;
constexpr int L_END = L_M + 512;
constexpr int F_SEL = 8, F_CNT = 9, F_TAG = 10, F_TOK = 11, F_SELOK = 12, F_XN = 13, F_QS = 14, F_LATE = 15, F_KD = 16, F_PR = 32,
              F_OR = 36, F_LINE = 7, F_VOTED = 6, F_P9 = 5, F_OFF = 40, F_G = 64, F_X = 80, F_GD = 96, F_OD = 100, F_PI = 120, F_SELV = 3, F_WT = 2, F_QA = 1;

extern "C" __global__ void __launch_bounds__(640, 3)
main_k(const unsigned short* __restrict__ x, const char* __restrict__ K, const char* __restrict__ V,
       const unsigned short* __restrict__ wq, const unsigned short* __restrict__ wo, const unsigned short* __restrict__ w1,
       const unsigned short* __restrict__ b1, const unsigned short* __restrict__ w2,
       const unsigned short* __restrict__ b2, char* __restrict__ ws, unsigned short* __restrict__ out) {
  __shared__ __attribute__((aligned(128))) char lds[L_END];
  unsigned* s_m = (unsigned*)(lds + L_M);
  const unsigned fm = lds_addr(lds) + L_M;
  u64 pc0;
  asm volatile("s_getpc_b64 %0" : "=s"(pc0));
#define RS 1
  const int tid = threadIdx.x, lane = tid & 63, pwave = __builtin_amdgcn_readfirstlane(tid >> 6);
  const int wave = RS == 0 ? pwave : pwave >= 2 ? (pwave - 2) ^ (RS == 3 ? 4 : 0) : RS != 2 ? 9 - pwave : 8 + pwave;
  if (EG == 5 && wave < 8) asm volatile("s_barrier" ::: "memory");
  const unsigned blk = blockIdx.x;
  const unsigned xs = blk & 7, m = blk >> 3;
  const unsigned j = (xs & 1) | ((xs >> 2) << 1);
  const unsigned h = (m >> 1) + ((xs & 2) << 3), bg = m & 1;
  const unsigned toff = h * 12 + bg * 14 + (h >> 4) * 7;
  auto slab_of = [=](unsigned k) { return (bg * 4 + ((k + h) & 3)) * NH + h; };

  const unsigned cgq = CM == 0 ? xs * 32 + m : CM == 1 ? blk : CM == 2 ? ((m >> 2) * 8 + xs) * 4 + (m & 3) : ((m >> 2) * 2 + (xs & 1) + (xs >> 1) * 16) * 4 + (m & 3);
  const unsigned ge = (xs >> 2) | ((xs & 1) << 1);
  const unsigned team = h * 2 + bg;
  const long long dvk = V - K;
  const unsigned stat = (ge * 0x55u) ^ ((m & 1) ? (STAT >> 2) : STAT);
  auto rot = [=](unsigned w) { return (unsigned)((w * 0x100000001ull) >> ((2 * m) & 31)); };
  const unsigned m3 = m % 3, x3 = (m + 1) % 3;
  auto cls_v = [=](unsigned sv) {
    const unsigned a = sv * 0x55555555u;
    return (u64)(((a ^ (NX == 4 ? 0x0C30C300u << (2 * m3) : rot(MIXA0)) ^ rot(MIXB)) & ~0xffu) | stat) | ((u64)(a ^ rot(MIXA) ^ rot(MIXB)) << 32);
  };
  auto cls_x = [=](unsigned d) {
    const unsigned a = d * 0x55555555u;
    return (u64)(a & (NX == 4 ? 0x0C30C300u << (2 * x3) : rot(MIXX0)) & ~0xffu) | ((u64)(a & rot(MIXX)) << 32);
  };
  auto addr = [=](unsigned t, u64 ck, u64 cv) -> const char* {
    const unsigned sg = (unsigned)(((t & 64) ? cv : ck) >> (((t & 48) ? 32 : 0) + 2 * (t & 15))) & 3;
    const unsigned n = ((t + toff) & 15) * 4 + (wave & 2);
    return K + ((t & 64) ? dvk : 0ll) + (blk_off(slab_of((t >> 4) & 3), n, sg) + (wave & 1) * 2048 + PB);
  };
  const unsigned voff = (lane & 15) * 16 + ((lane >> 4) & 1) * 256 + (lane >> 5) * 32768;
  const unsigned pc = ((SP ? 0 : wave) ^ (m >> 2)) & 1;
  auto probe_addr = [=]() -> const char* {
    const unsigned rnd = ((unsigned)__builtin_amdgcn_s_memrealtime() + (blk * 2 + wave) * 0x85EBCA6Bu) * 0x9E3779B1u;
    const unsigned pn = ((toff + 8 + (rnd >> 29)) & 15) * 4;
    return K + ((m & 1) ? dvk : 0ll) + (blk_off(PSL ? (PSL == 1 ? 0u : xs) : slab_of(0), PSL ? (rnd >> 26) : pn + ((rnd >> 27) & 3), ge ^ (m & 2) ^ pc) + ((rnd >> 21) & 31) * 256);
  };
  const unsigned vslot = (SP ? (m >> 4) * 64 + (m & 3) * 16 + ((m >> 3) & 1) * 8 + xs : (m >> 3) * 64 + (m & 3) * 16 + ((m >> 2) & 1) * 8 + xs) * 16 + pc * 8;
  auto finish = [&](unsigned tag) {
#pragma nounroll
    while (!flag_four(fm + F_GD * 4)) NAP(1);
    const f32x2 sg = *(const f32x2*)(s_m + F_G + 2 * (lane & 7)), sx = *(const f32x2*)(s_m + F_X + 2 * (lane & 7));
    const char* in = ws + WS_Y + (size_t)blk * 4096;
    float y0, y1;
    PRIO(3);
    {
      u32x2 gy[3];
#pragma nounroll
      for (unsigned spin = 0; spin < SPIN_MAX; spin++) {
        if (W9 == 1) NAP(YNAP);
        if (YL) {
          const char* i2 = ws + WS_Y2 + (size_t)(blk >> 1) * Y2S + (blk & 1) * 64;
          const unsigned o = (lane >> 3) * 128 + (lane & 7) * 8;
          ld8_dev<0>(gy[0], o, i2); ld8_dev<1024>(gy[1], o, i2); ld8_dev<2048>(gy[2], o, i2);
        } else {
          ld8_dev<0>(gy[0], lane * 8, in); ld8_dev<512>(gy[1], lane * 8, in); ld8_dev<1024>(gy[2], lane * 8, in);
        }
        unsigned bad = 0;
#pragma unroll
        for (int i = 0; i < 3; i++) { waitv<0>(gy[i]); bad |= gy[i][1] ^ tag; }
        if (__all(bad == 0)) break;
      }
      y0 = bf_lo(gy[0][0]) + bf_lo(gy[1][0]) + bf_lo(gy[2][0]);
      y1 = bf_hi(gy[0][0]) + bf_hi(gy[1][0]) + bf_hi(gy[2][0]);
      STAMP(42);
    }
    if (W9) {
#pragma nounroll
      while (YW ? !flag_one(fm + (F_OR + 3) * 4) : !flag_four(fm + (F_OD + 12) * 4)) NAP(4);
    }
    Fly fly;
    {
      unsigned g0, g1;
      poll2(fly, g0, g1, lane * 16, in + 3072, __builtin_amdgcn_readfirstlane(tag), SPIN_MAX);
      y0 += u2f(g0); y1 += u2f(g1);
      STAMP(43);
    }
    y0 += dpp<0x128>(y0); y1 += dpp<0x128>(y1);
    y0 = fold16(y0, y0); y1 = fold16(y1, y1);
    y0 = fold32(y0, y0); y1 = fold32(y1, y1);
    {
      const unsigned res = pack2(sx[0] + rbf(rbf(y0) * sg[0]), sx[1] + rbf(rbf(y1) * sg[1]));
      if (lane < 8) asm volatile("global_store_dword %0, %1, %2" ::"v"(lane * 4), "v"(res), "s"(out + blk * 16) : "memory");
    }
    STAMP(20);
    landed(fly);
  };
  Ring ra;
  if (wave < 8 && (EG < 5 || wave >= 4)) {
    char* s_xn = lds + L_P;
    u32x4 xr, xr2, wx[4];
    if (EG >= 5) {
      ld16(xr, lane * 32, x + (wave - 4) * 2 * DM);
      ld16(xr2, lane * 32 + 16, x + (wave - 4) * 2 * DM);
    } else
      ld16(xr, lane * 16, x + wave * DM);
    {
      const u32x4 z = {0, 0, 0, 0};
#pragma unroll
      for (int i = 0; i < 8; i++) { ra.r[i] = z; asm volatile("" : "+v"(ra.r[i])); }
#pragma unroll
      for (int i = 0; i < 4; i++) { wx[i] = z; asm volatile("" : "+v"(wx[i])); }
    }
    const unsigned short* wob = wo + (size_t)(h * DH + 4 + (wave & 3) * 32) * DM + j * 128;
#define WOV ((lane >> 4) * 8192 + (lane & 15) * 16)
    auto wo_issue = [&]() {
#define WOL(i) asm volatile("global_load_dwordx4 %0, %1, %2 offset:%3 nt" : "+v"(ra.r[i]) : "v"(WOV), "s"(wob), "i"(i * 1024 - 4096));
      WOL(0) WOL(1) WOL(2) WOL(3) WOL(4) WOL(5) WOL(6) WOL(7)
#undef WOL
    };
    if (wave >= 4) {
#pragma unroll
      for (int i = 0; i < 4; i++)
        asm volatile("global_load_dwordx4 %0, %1, %2" WQP : "+v"(wx[i]) : "v"((lane >> 3) * (NH * DH * 2) + (lane & 7) * 16), "s"(wq + (size_t)((cgq & 3) * 128 + (wave & 3) * 32 + i * 8) * (NH * DH) + (cgq >> 2) * 64));
    }
    asm volatile("" : "+s"(wob));
    if (WOE == 1 && wave >= 4) wo_issue();
    const char* a0 = addr(0, stat, 0);
    if (EG) asm volatile("" : "+s"(a0));
    if (EG == 4 && wave < 4) {
      issue8w<8, 0>(ra, voff, a0);
    }
    if (EG != 5) wg_barrier();
    if (EG && EG < 4 && wave < 4) {
      if (EG == 2) {
#pragma nounroll
        while (!flag_one(fm + F_P9 * 4)) NAP(1);
      }
      issue8w<8, 0>(ra, voff, a0);
    }
    if (WOE == 7 && wave >= 4) wo_issue();
    if (wave == 7) STAMP(33);
    {
      float xv[8], xn[8];
      if (wave >= 4) asm volatile("s_waitcnt vmcnt(%0)" ::"i"(WOE == 1 || WOE == 7 ? 12 : 4)); else asm volatile("s_waitcnt vmcnt(%0)" ::"i"(EG ? 8 : 0));
      asm volatile("" : "+v"(xr));
      if (EG >= 5) asm volatile("" : "+v"(xr2));
      if (wave == (EG >= 5 ? 4 : 0)) STAMP(44);
      if (EG >= 5) {
        float xw[16], xm[16];
#pragma unroll
        for (int k = 0; k < 4; k++) { xw[2 * k] = bf_lo(xr[k]); xw[2 * k + 1] = bf_hi(xr[k]); xw[8 + 2 * k] = bf_lo(xr2[k]); xw[9 + 2 * k] = bf_hi(xr2[k]); }
        ln16(xw, xm);
        u32x4* d = (u32x4*)(s_xn + (wave - 4) * 2048 + lane * 32);
        d[0] = u32x4{pack2(xm[0], xm[1]), pack2(xm[2], xm[3]), pack2(xm[4], xm[5]), pack2(xm[6], xm[7])};
        d[1] = u32x4{pack2(xm[8], xm[9]), pack2(xm[10], xm[11]), pack2(xm[12], xm[13]), pack2(xm[14], xm[15])};
      } else {
#pragma unroll
        for (int k = 0; k < 4; k++) { xv[2 * k] = bf_lo(xr[k]); xv[2 * k + 1] = bf_hi(xr[k]); }
        ln8(xv, xn);
        *(u32x4*)(s_xn + wave * 1024 + lane * 16) = u32x4{pack2(xn[0], xn[1]), pack2(xn[2], xn[3]), pack2(xn[4], xn[5]), pack2(xn[6], xn[7])};
      }
    }
    if (wave == (EG >= 5 ? 4 : 0)) STAMP(51);
    if (wave >= 4) {
      PRIO(3);
      if (WOE == 9) {
#pragma nounroll
        while (!flag_one(fm + F_VOTED * 4)) NAP(1);
      }
      if (WOE == 8 || WOE == 9) wo_issue();
      const unsigned im = fm - L_M + L_P + 16384, g = lane >> 4;
      unsigned wa = im + (wave & 3) * 4096 + (lane >> 3) * 128 + ((lane & 7) ^ ((g & 1) * 2)) * 16, wb = (wa ^ 64) + 1024;
      unsigned rd = im + (g * 8 + ((lane & 15) >> 2)) * 128 + (((wave & 3) * 2 + ((lane >> 1) & 1)) ^ ((lane >> 2) & 2) ^ ((g & 1) * 4)) * 16 + (lane & 1) * 8;
      unsigned rx = fm - L_M + L_P + (lane & 7) * 1024 + (cgq & 3) * 256 + g * 16;
      const bool soon = (lane & 3) == ((cgq >> 3) & 3);
      unsigned so = soon ? (unsigned)WS_Q + (cgq >> 3) * 8192 + (lane & 4) * 1024 + (cgq & 3) * 1024 + ((cgq >> 2) & 1) * 512 + (wave & 3) * 128 + g * 32 + (lane & 8) * 2
                         : (unsigned)WS_QN + (cgq >> 3) * 16384 + (lane & 7) * 2048 + (cgq & 3) * 512 + ((cgq >> 2) & 1) * 256 + (wave & 3) * 64 + g * 16;
      char* qd = ws;
      asm volatile("" : "+v"(wa), "+v"(wb), "+v"(rd), "+v"(rx), "+v"(so), "+s"(qd));
      unsigned tag;
#pragma nounroll
      while (true) {
        u32x2 tv;
        asm volatile("ds_read_b64 %0, %1\n\ts_waitcnt lgkmcnt(0)" : "=v"(tv) : "v"(fm + F_TAG * 4) : "memory");
        const unsigned t0 = tv[0], t1 = tv[1];
        tag = t0;
        if (__builtin_amdgcn_readfirstlane(t1)) break;
      }
      if (wave == 4) STAMP(1);
      asm volatile("s_waitcnt vmcnt(%6)\n\tds_write_b128 %0, %2\n\tds_write_b128 %1, %3\n\tds_write_b128 %0, %4 offset:2048\n\tds_write_b128 %1, %5 offset:2048"
                   : : "v"(wa), "v"(wb), "v"(wx[0]), "v"(wx[1]), "v"(wx[2]), "v"(wx[3]), "i"(WOE == 1 || WOE >= 7 ? 8 : 0) : "memory");
      if (lane == 0) asm volatile("ds_add_u32 %0, %1" ::"v"(fm + F_XN * 4), "v"(1u) : "memory");
      if (wave == 4) STAMP(45);
      if (wave == 7) STAMP(17);
      if (WOE == 2) wo_issue();
#pragma nounroll
      while (flag_val(fm + F_XN * 4) != 4) { }
      u32x2 wr[8];
      u32x4 xa[4];
#pragma unroll
      for (int v = 0; v < 4; v++)
        asm volatile("ds_read_b64_tr_b16 %0, %3 offset:%5\n\tds_read_b64_tr_b16 %1, %3 offset:%6\n\tds_read_b128 %2, %4 offset:%7"
                     : "=&v"(wr[2 * v]), "=&v"(wr[2 * v + 1]), "=&v"(xa[v]) : "v"(rd), "v"(rx), "i"(v * 4096), "i"(v * 4096 + 512), "i"(v * 64) : "memory");
      asm volatile("s_waitcnt lgkmcnt(0)" : "+v"(wr[0]), "+v"(wr[1]), "+v"(wr[2]), "+v"(wr[3]), "+v"(wr[4]), "+v"(wr[5]), "+v"(wr[6]), "+v"(wr[7]),
                   "+v"(xa[0]), "+v"(xa[1]), "+v"(xa[2]), "+v"(xa[3]));
      f32x4 dq[2];
#pragma unroll
      for (int v = 0; v < 4; v++) {
        const u32x4 wf = {wr[2 * v][0], wr[2 * v][1], wr[2 * v + 1][0], wr[2 * v + 1][1]};
        dq[v & 1] = __builtin_amdgcn_mfma_f32_16x16x32_bf16(__builtin_bit_cast(bf16x8, wf), __builtin_bit_cast(bf16x8, xa[v]), v < 2 ? f32x4{0.f, 0.f, 0.f, 0.f} : dq[v & 1], 0, 0, 0);
      }
      const f32x4 dd = dq[0] + dq[1];
      if (soon || !(lane & 8))
        st16_dev(qd, so, u32x4{f2u(lane & 8 ? dd[2] : dd[0]), soon ? tag : f2u(dd[1]), f2u(!soon ? dd[2] : lane & 8 ? dd[3] : dd[1]), soon ? tag : f2u(dd[3])});
      if (lane == 0) asm volatile("ds_add_u32 %0, %1" ::"v"(fm + F_CNT * 4), "v"(1u) : "memory");
      if (WOE == 4) wo_issue();
      if (wave == 4) STAMP(15);
      if (wave == 7) { STAMP(19); STAMP(13); }
      if (WOE == 5 || WOE == 6) {
#pragma nounroll
        while (!flag_one(fm + (WOE == 5 ? F_PI + (wave & 3) : F_SELOK) * 4)) NAP(4);
        wo_issue();
      }
    }
  }

  if (wave < 4) {
    if (EG == 6) asm volatile("s_barrier" ::: "memory");
    PRIO(2);
    const int r = lane >> 4, c = lane & 15;
    const unsigned phi = ((m & 15) * 4 + wave) * PPH / 4;
    const int slope = TB < 60 ? (63 * PPH / 8 + PEND - (int)phi) * 256 / (TB < 60 ? 60 - TB : 1) : 0;
    Ring rb;
    u32x4 q;
    u64 ck = stat, cv = 0;
    unsigned short* sdst = (unsigned short*)(lds + L_SC) + wave * 32 + r * 8 + (c >> 1);
    {
      Ring rc, rd;
      const char* a0 = addr(0, ck, cv);
      asm volatile("" : "+s"(a0));
      if (GO == 0) {
#pragma nounroll
        while (!flag_one(fm + F_QS * 4)) NAP(1);
      } else if (GO == 1) {
#pragma nounroll
        while (flag_val(fm + F_VOTED * 4) != 2 - SP) NAP(1);
      } else if ((GO == 3 || GO == 4) && (!EG || EG >= 5)) {
#pragma nounroll
        while (!flag_one(fm + F_P9 * 4)) NAP(1);
      }
      if ((!EG || EG >= 5) && wave == 0) STAMP(3);
      if (GO == 4) {
        const u32x4 z = {0, 0, 0, 0};
#pragma unroll
        for (int i = 0; i < 8; i++) ra.r[i] = z;
        ldnt<ioff(0)>(ra.r[0], voff, a0); ldnt<ioff(1)>(ra.r[1], voff, a0);
        if (G4N > 2) { ldnt<ioff(2)>(ra.r[2], voff, a0); ldnt<ioff(3)>(ra.r[3], voff, a0); }
#pragma nounroll
        while (flag_val(fm + F_VOTED * 4) != 2 - SP) NAP(1);
        if (G4N <= 2) { ldnt<ioff(2)>(ra.r[2], voff, a0); ldnt<ioff(3)>(ra.r[3], voff, a0); }
        ldnt<ioff(4)>(ra.r[4], voff, a0); ldnt<ioff(5)>(ra.r[5], voff, a0); ldnt<ioff(6)>(ra.r[6], voff, a0); ldnt<ioff(7)>(ra.r[7], voff, a0);
      } else
      if (!EB && (!EG || EG >= 5)) issue8w<WIN, 0>(ra, voff, a0);
      issue8w<WIN, 8>(rb, voff, addr(1, ck, cv));
      if (wave == 0) STAMP(7);
      issue8w<WIN2, 16>(rc, voff, addr(2, ck, cv));
      if (wave == 0) STAMP(11);
      issue8w<WIN3, 24>(rd, voff, addr(3, ck, cv));
      if (WOE == 5) flag_set(fm + (F_PI + wave) * 4);
      if (wave == 0) STAMP(46);
#pragma nounroll
      while (!flag_one(fm + F_SELV * 4)) NAP(1);
      { const unsigned s = __builtin_amdgcn_readfirstlane(s_m[F_SEL]); cv = cls_v(s >> 2); ck = cv ^ cls_x((s ^ (s >> 2)) & 3); }
      asm volatile("" : "+s"(ck), "+s"(cv));
#pragma nounroll
      while (true) {
        unsigned f;
        asm volatile("ds_read_b32 %0, %2\n\tds_read_b128 %1, %3\n\ts_waitcnt lgkmcnt(0)" : "=&v"(f), "=&v"(q) : "v"(fm + F_SELOK * 4), "v"(fm - L_M + L_Q + c * 16) : "memory");
        if (__builtin_amdgcn_readfirstlane(f)) break;
        NAP(1);
      }
      if (wave == 0) STAMP(47);
      k_piece<15, 0, true>(ra, q, voff, addr(4, ck, cv), c, sdst);
      if (wave == 0) STAMP(55);
      k_piece<15, 0, true>(rb, q, voff, addr(5, ck, cv), c, sdst + 128);
      if (wave == 0) STAMP(28);
      k_piece<31, 1, false>(rc, q, 0, nullptr, c, sdst + 256);
      k_piece<23, 1, false>(rd, q, 0, nullptr, c, sdst + 384);
      if (wave == 0) STAMP(58);
    }
    for (unsigned t = P0; t < 64; t += 2) {
      if ((t & 15) == 0) {
#pragma nounroll
        while (!flag_one(fm + F_QA * 4)) NAP(1);
        q = *(const u32x4*)(lds + L_Q + t * 16 + c * 16);
      }
      if (t >= PT0) pace(fm + F_LINE * 4, fm + (F_OFF + wave) * 4, t, phi);
      k_piece<15, 0, true>(ra, q, voff, addr(t + 2, ck, cv), c, sdst + t * 128);
      k_piece<15, 0, true>(rb, q, voff, addr(t + 3, ck, cv), c, sdst + t * 128 + 128);
      if (t == P0 && wave == 0) STAMP(29);
      if ((t & 15) == 14) { flag_set(fm + (F_KD + (t >> 4) * 4 + wave) * 4); if (wave == 0) STAMP(4 + (t >> 4) * 2); }
    }
    float acc[8] = {0, 0, 0, 0, 0, 0, 0, 0};
    const unsigned* pp = (const unsigned*)(lds + L_P) + wave * 32 + r * 8;
#define FLUSH(k)                                                                                   \
    { *(f32x2*)((float*)(lds + L_O) + ((k) * 4 + wave) * 128 + c * 8 + r * 2) = fold_rows(acc);    \
      flag_set(fm + (F_OD + (k) * 4 + wave) * 4);                                                  \
      if (wave == 0 && (k) < 4) STAMP(12 + (k) * 2);                                               \
      _Pragma("unroll") for (int i = 0; i < 8; i++) acc[i] = 0.f; }
    for (unsigned t = 0; t < 62; t += 2) {
      if ((t & 15) == 0) {
#pragma nounroll
        while (!flag_one(fm + (F_PR + (t >> 4)) * 4)) NAP(1);
        if (wave == 0 && t < 16) STAMP(5 + (t >> 4) * 2);
      }
      const Wts pa = wts(pp + t * 128), pb = wts(pp + t * 128 + 128);
      pace(fm + F_LINE * 4, fm + (F_OFF + wave) * 4, 64 + t, phi + (t > TB ? ((int)(t - TB) * slope) >> 8 : 0));
      v_piece<0>(ra, pa, acc, voff, addr(64 + t + 2, ck, cv));
      v_piece<0>(rb, pb, acc, voff, addr(64 + t + 3, ck, cv));
      if ((t & 15) == 14 || t == P0 - 2) { const unsigned part = t == P0 - 2 ? 4 : t >> 4; FLUSH(part) }
      if (t == LATE) { flag_set(fm + F_LATE * 4); if (wave == 0) STAMP(57); }
    }
    {
      const Wts pa = wts(pp + 62 * 128), pb = wts(pp + 63 * 128);
      v_piece<1>(ra, pa, acc, voff, nullptr);
      v_piece<2>(rb, pb, acc, voff, nullptr);
    }
    FLUSH(3)
#undef FLUSH
  } else if (wave < 8) {
    const int hw = wave - 4;
    PRIO(0);
    {
      const unsigned bc = blk >> 5, cg = blk & 31;
      float xv[8], xn[8], hh[8];
      u32x4 xg;
      unsigned b2r;
#pragma nounroll
      while (!flag_one(fm + F_SELOK * 4)) NAP(8);
      {
        const unsigned tw = fm - L_M + L_P + hw * 1024 + lane * 16, tr = fm - L_M + L_P + (lane >> 2) * 256 + hw * 64 + (lane & 3) * 16;
        landed(ra);
#pragma unroll
        for (int i = 0; i < 8; i++) asm volatile("ds_write_b128 %0, %1 offset:%2" ::"v"(tw), "v"(ra.r[i]), "i"(i * 4096) : "memory");
        if (lane == 0) asm volatile("ds_add_u32 %0, %1" ::"v"(fm + F_WT * 4), "v"(1u) : "memory");
#pragma nounroll
        while (flag_val(fm + F_WT * 4) < 4) NAP(2);
        if (hw == 0 && lane == 0) st8_dev(ws + WS_QF, cgq * 8, u32x2{0, flag_val(fm + F_TAG * 4)});
#pragma unroll
        for (int i = 0; i < 8; i++) asm volatile("ds_read_b128 %0, %1 offset:%2" : "=v"(ra.r[i]) : "v"(tr), "i"(i * 4096) : "memory");
        asm volatile("s_waitcnt lgkmcnt(0)" : "+v"(ra.r[0]), "+v"(ra.r[1]), "+v"(ra.r[2]), "+v"(ra.r[3]), "+v"(ra.r[4]), "+v"(ra.r[5]), "+v"(ra.r[6]), "+v"(ra.r[7]));
        if (lane == 0) asm volatile("ds_add_u32 %0, %1" ::"v"(fm + F_WT * 4), "v"(1u) : "memory");
      }
      PRIO(3);
      ld16(xg, lane * 16, x + bc * DM);
      asm volatile("global_load_ushort %0, %1, %2" : "=v"(b2r) : "v"(0u), "s"(b2));
      PRIO(0);
      waitv<0>(xg);
      waitv<0>(b2r);
#pragma unroll
      for (int k = 0; k < 4; k++) { xv[2 * k] = bf_lo(xg[k]); xv[2 * k + 1] = bf_hi(xg[k]); }
      ln8(xv, xn);
      ln8(xn, hh);
      float hv[4], ya[4], xo[4];
      const int src = cg * 2 + (hw >> 1);
#pragma unroll
      for (int i = 0; i < 4; i++) {
        const float ha = __shfl(hh[i], src), hb = __shfl(hh[4 + i], src);
        hv[i] = (hw & 1) ? hb : ha;
        const float xa = __shfl(xv[i], src), xb = __shfl(xv[4 + i], src);
        xo[i] = (hw & 1) ? xb : xa;
        ya[i] = 0.f;
      }
#pragma nounroll
      for (int ch = 0; ch < 4; ch++) {
        u32x4 ga, gb, gc;
        const unsigned o = ch * 1024 + (ch < 3 ? lane : lane & 31) * 16;
        PRIO(3);
        ld16(ga, o, w1); ld16(gb, o, b1); ld16(gc, o, w2);
        PRIO(0);
        waitv<0>(ga); waitv<0>(gb); waitv<0>(gc);
        if (ch == 3 && lane >= 32) gc = u32x4{0, 0, 0, 0};
#pragma unroll
        for (int e = 0; e < 8; e++) {
          const float a = (e & 1) ? bf_hi(ga[e >> 1]) : bf_lo(ga[e >> 1]);
          const float bb = (e & 1) ? bf_hi(gb[e >> 1]) : bf_lo(gb[e >> 1]);
          const float w = (e & 1) ? bf_hi(gc[e >> 1]) : bf_lo(gc[e >> 1]);
#pragma unroll
          for (int i = 0; i < 4; i++) {
            const float u = rbf(rbf(hv[i] * a) + bb);
            const float zz = u * fmaf(u * u, 0.044715f, 1.f) * (-2.f * 0.7978845608028654f * LOG2E);
            const float g = rbf(u * __builtin_amdgcn_rcpf(1.f + __builtin_amdgcn_exp2f(zz)));
            ya[i] = fmaf(g, w, ya[i]);
          }
        }
      }
#pragma unroll
      for (int i = 0; i < 4; i++) {
        const float yy = rbf(rbf(wave_sum(ya[i])) + bf_lo(b2r));
        const float g = rbf(yy / (1.f + __expf(-yy)));
        if (lane == 0) {
          ((float*)s_m)[F_G + hw * 4 + i] = g;
          ((float*)s_m)[F_X + hw * 4 + i] = xo[i];
        }
      }
      flag_set(fm + (F_GD + hw) * 4);
    }
    if (hw == 0) STAMP(26);
    const unsigned tag = s_m[F_TAG];
    const int rs = lane >> 2, cc = lane & 3;
    u32x4 wv[8];
    if (!WOE) {
      PRIO(3);
#pragma unroll
      for (int i = 0; i < 8; i++) ld16o(wv[i], rs * 8192 + cc * 16, wo + (size_t)(h * DH + i) * DM + j * 128 + hw * 32);
      PRIO(0);
#pragma unroll
      for (int i = 0; i < 8; i++) waitv<0>(wv[i]);
    } else {
#pragma unroll
      for (int i = 0; i < 8; i++) { waitv<0>(ra.r[i]); wv[i] = ra.r[i]; }
    }
#pragma unroll
    for (int i = 0; i < 8; i += 2)
#pragma unroll
      for (int e = 0; e < 4; e++) {
        const unsigned a = wv[i][e], b = wv[i + 1][e];
        wv[i][e] = __builtin_amdgcn_perm(b, a, 0x05040100u);
        wv[i + 1][e] = __builtin_amdgcn_perm(b, a, 0x07060302u);
      }
    if (hw == 0) STAMP(25);
#pragma nounroll
    for (unsigned k = 0; k < 4; k++) {
      if (k < 3) {
#pragma nounroll
        while (!flag_one(fm + F_OR * 4)) NAP(16);
        if (YLW && k == 0) {
#pragma nounroll
          while (!flag_one(fm + F_LATE * 4) && !reached(fm + F_LINE * 4, YLW)) NAP(1);
        }
      } else {
#pragma nounroll
        while (!flag_four(fm + (F_OD + 12) * 4)) NAP(16);
        PRIO(3);
      }
      u32x4 ob;
#pragma nounroll
      while (true) {
        unsigned f;
        asm volatile("ds_read_b32 %0, %2\n\tds_read_b128 %1, %3\n\ts_waitcnt lgkmcnt(0)" : "=&v"(f), "=&v"(ob)
                     : "v"(fm + (F_OR + (k == 3 ? 3 : 0)) * 4), "v"(fm - L_M + L_OB + k * 256 + rs * 16) : "memory");
        if (__builtin_amdgcn_readfirstlane(f)) break;
      }
      float y[8];
#pragma unroll
      for (int e = 0; e < 4; e++) {
        y[2 * e] = dot2(wv[0][e], ob[0], 0.f); y[2 * e + 1] = dot2(wv[1][e], ob[0], 0.f);
#pragma unroll
        for (int i = 1; i < 4; i++) { y[2 * e] = dot2(wv[2 * i][e], ob[i], y[2 * e]); y[2 * e + 1] = dot2(wv[2 * i + 1][e], ob[i], y[2 * e + 1]); }
      }
      const f32x2 yy = fold_rows(y);
      float y0 = yy[0], y1 = yy[1];
      y0 += dpp<0x124>(y0); y1 += dpp<0x124>(y1);
      y0 += dpp<0x128>(y0); y1 += dpp<0x128>(y1);
      PRIO(3);
      if (YL && k < 3) {
        const unsigned pk = pack2(y0, y1);
        const auto sw = __builtin_amdgcn_permlane16_swap(pk, pk, false, false);
        const unsigned s0 = sw[0], s1 = sw[1];
        if ((lane & 28) == 0)
          st16_dev(ws + WS_Y2 + (size_t)(((slab_of(k) >> 5) * 16 + j * 4 + hw) * (Y2S / 128) + k * 8 + (h >> 2)) * 128, cc * 32 + (lane >> 4) * 8, u32x4{s0, tag, s1, tag});
      } else if ((lane & 12) == 0) {
        char* yb = ws + WS_Y + (size_t)((slab_of(k) >> 5) * 32 + j * 8 + hw * 2) * 4096;
        if (k == 3) st16_dev(yb + 3072 + (h >> 2) * 128, (cc >> 1) * 4096 + (cc & 1) * 64 + (lane >> 4) * 16, u32x4{f2u(y0), tag, f2u(y1), tag});
        else st8_dev(yb + k * 512 + (h >> 2) * 64, (cc >> 1) * 4096 + (cc & 1) * 32 + (lane >> 4) * 8, u32x2{pack2(y0, y1), tag});
      }
      PRIO(0);
      if (hw == 0 && k >= 2) STAMP(36 + k);
    }
  } else if (wave == 9) {
    PRIO(3);
    unsigned cpf[CPF + 1];
#pragma unroll
    for (int i = 0; i < CPF; i++)
      asm volatile("global_load_dword %0, %1, %2" : "=v"(cpf[i]) : "v"(lane * 128 + i * 8192), "s"(pc0 & ~255ull));
    unsigned kpf;
    if (KPF == 1) asm volatile("global_load_dword %0, %1, %2" : "=v"(kpf) : "v"(0u), "s"(K + ((size_t)slab_of(0) << 21) + xs * 16384 + 128));
    const char* pa = probe_addr();
#define EGBAR "s_barrier\n\t"
    unsigned la, ep;
    {
      const char* tb = K + ((m & 1) ? dvk : 0ll);
      const char* pb = tb + ((unsigned)(pa - tb) ^ (8192u + 1024u));
      unsigned ev, wa, wb;
      u64 t1, t2;
      if (DP == 1) asm volatile("global_load_dword %0, %1, %2 sc1" : "=v"(ev) : "v"(0u), "s"(ws + WS_EPOCH));
      asm volatile("s_load_dword %0, %4, 0x0\n\ts_load_dword %1, %5, 0x0\n\t" EGBAR "s_waitcnt lgkmcnt(1)\n\ts_memtime %2\n\ts_waitcnt lgkmcnt(0)\n\ts_memtime %3\n\ts_waitcnt lgkmcnt(0)"
                   : "=&s"(wa), "=&s"(wb), "=&s"(t1), "=&s"(t2) : "s"(pb), "s"(pa) : "memory");
      la = (unsigned)(t2 - t1);
      la = min(la, (unsigned)DCL) + 1;
      if (EG < 2) wg_barrier();
      if (!EG || EG == 2 || EG >= 5) flag_set(fm + F_P9 * 4);
    }
    unsigned tag;
#pragma nounroll
    while (true) {
      u32x2 tv;
      asm volatile("ds_read_b64 %0, %1\n\ts_waitcnt lgkmcnt(0)" : "=v"(tv) : "v"(fm + F_TAG * 4) : "memory");
      const unsigned t0 = tv[0], t1 = tv[1];
      tag = __builtin_amdgcn_readfirstlane(t0);
      if (__builtin_amdgcn_readfirstlane(t1)) break;
    }
#pragma unroll
    for (int i = 0; i < CPF; i++) waitv<0>(cpf[i]);
    if (lane == 0) {
      st8_dev(ws + WS_VOTE, vslot, u32x2{SP ? la | ((unsigned)__builtin_amdgcn_s_memrealtime() << 12) : la, tag});
      asm volatile("ds_add_u32 %0, %1" ::"v"(fm + F_VOTED * 4), "v"(1u) : "memory");
    }
    STAMP(34);
    if (KPF == 1) waitv<0>(kpf);
    {
    NAP(VNAP);
    u32x4 gv[2];
#pragma nounroll
    for (unsigned spin = 0; spin < SPIN_MAX; spin++) {
      ld16_dev<0>(gv[0], lane * 16, ws + WS_VOTE); ld16_dev<1024>(gv[1], lane * 16, ws + WS_VOTE);
      waitv<0>(gv[0]); waitv<0>(gv[1]);
      if (__all(((gv[0][1] ^ tag) | (gv[0][3] ^ tag) | (gv[1][1] ^ tag) | (gv[1][3] ^ tag)) == 0)) break;
    }
    unsigned tstart = (unsigned)__builtin_amdgcn_s_memrealtime();
    if (CT) tstart -= ((tstart - (__builtin_amdgcn_readlane(gv[0][0], 0) >> 12)) & 0xfffffu) - 430;
    unsigned sk, sv;
    {
      unsigned sa = 0, sb = 0;
#pragma unroll
      for (int i = 0; i < 2; i++) {
        const unsigned la = gv[i][0] & 0xfffu, lb = gv[i][2] & 0xfffu;
        sa += (la << 8) | (la != 0); sb += (lb << 8) | (lb != 0);
      }
#define ROWSUM(v) v += __builtin_amdgcn_mov_dpp(v, 0xB1, 0xf, 0xf, true); v += __builtin_amdgcn_mov_dpp(v, 0x4E, 0xf, 0xf, true); \
                  v += __builtin_amdgcn_mov_dpp(v, 0x141, 0xf, 0xf, true); v += __builtin_amdgcn_mov_dpp(v, 0x140, 0xf, 0xf, true);
      ROWSUM(sa) ROWSUM(sb)
#undef ROWSUM
      const unsigned ma = (sa & 255u) ? (unsigned)((float)(sa >> 8) * 16.f * __builtin_amdgcn_rcpf((float)(sa & 255u))) : 0xffffffu;
      const unsigned mb = (sb & 255u) ? (unsigned)((float)(sb >> 8) * 16.f * __builtin_amdgcn_rcpf((float)(sb & 255u))) : 0xffffffu;
      unsigned dc[2];
#pragma unroll
      for (int T = 0; T < 2; T++) {
        const unsigned a0 = __builtin_amdgcn_readlane(ma, T * 16), a2 = __builtin_amdgcn_readlane(ma, 32 + T * 16);
        const unsigned b0 = __builtin_amdgcn_readlane(mb, T * 16), b2 = __builtin_amdgcn_readlane(mb, 32 + T * 16);
        const bool c1 = b0 + b2 < a0 + a2;
        dc[T] = (c1 ? 1u : 0u) | ((DP ? (c1 ? a2 > a0 : b2 > b0) : (c1 ? b2 < b0 : a2 < a0)) ? 2u : 0u);
      }
      sv = ge ^ dc[1];
      sk = ((dc[0] ^ dc[1]) & 1) ? sv ^ 3 : sv;
      if (lane == 0) {
        s_m[F_SEL] = sk | (sv << 2);
        if (PINI) s_m[F_LINE] = tstart + PINI;
      }
      flag_set(fm + F_SELV * 4);
    }
    STAMP(50);
    if (blk == 0 && lane == 0) asm volatile("global_store_dword %0, %1, %2 sc1" ::"v"(0u), "v"(tag), "s"(ws + WS_EPOCH) : "memory");
    }
    PRIO(0);
    if (TPF) {
      unsigned d[7];
#pragma unroll
      for (int i = 0; i < 7; i++)
        asm volatile("s_load_dword %0, %1, 0x0" : "=s"(d[i]) : "s"(K + (i >= 3 ? dvk : 0ll) + ((size_t)slab_of((i + 1) & 3) << 21) + blk * 128) : "memory");
      asm volatile("s_waitcnt lgkmcnt(0)" : "+s"(d[0]), "+s"(d[1]), "+s"(d[2]), "+s"(d[3]), "+s"(d[4]), "+s"(d[5]), "+s"(d[6]));
    }
    unsigned cur = 0;
    bool have = false;
    if (PINI) {
#pragma nounroll
      while (!flag_one(fm + F_SELOK * 4)) NAP(16);
      cur = flag_val(fm + F_LINE * 4);
      have = true;
    }
#pragma nounroll
    while (!flag_one(fm + F_OR * 4)) {
      const unsigned nxt = (((unsigned)__builtin_amdgcn_s_memrealtime() >> PSH) + 1) << PSH, rr = (nxt >> PSH) & 63;
      char* area = ws + WS_PR + rr * PAS;
      unsigned o;
      {
#pragma nounroll
        while ((int)((unsigned)__builtin_amdgcn_s_memrealtime() - nxt + 40) < 0) NAP(8);
        u32x4 o4;
        asm volatile("ds_read_b128 %0, %1\n\ts_waitcnt lgkmcnt(0)" : "=v"(o4) : "v"(fm + F_OFF * 4) : "memory");
        o = o4[0] + (unsigned)((int)((o4[1] - o4[0]) + (o4[2] - o4[0]) + (o4[3] - o4[0])) >> 2);
        if (!__builtin_amdgcn_readfirstlane(o4[0] & o4[1] & o4[2] & o4[3] & 1u)) { NAP(32); continue; }
      }
      if (PWR && ((m + rr * PROT) & (PM - 1)) == 0) {
        PRIO(3);
#pragma nounroll
        while ((int)((unsigned)__builtin_amdgcn_s_memrealtime() - nxt) < 0) NAP(1);
        if (lane == 0) st8_dev(area, ((m / PM) * 8 + xs) * 8, u32x2{o, tag});
        PRIO(0);
      }
#pragma nounroll
      while ((int)((unsigned)__builtin_amdgcn_s_memrealtime() - nxt - PLAG) < 0) NAP(8);
      u32x2 g;
      PRIO(3);
      ld8_dev(g, (lane & (256 / PM - 1)) * 8, area);
      PRIO(0);
      waitv<0>(g);
      const bool ok = g[1] == tag;
      if (PACE == 3 && have) o = cur;
      const float d = ok ? (float)min(max((int)(g[0] - o), have ? -PCN : -PCL), have ? PCP : PCL) : 0.f;
      const unsigned n = __popcll(__ballot(ok));
      const float sum = wave_sum(d);
      if (n >= 32) {
        const float mean = sum * __builtin_amdgcn_rcpf((float)n);
        const unsigned line = PACE == 2 ? o + (int)mean - PDEL : o + (int)(have ? (mean + PS) * PG : mean + (PS - PF1));
        if (PACE == 3) { cur = line; have = true; }
        asm volatile("ds_write_b32 %0, %1" ::"v"(fm + F_LINE * 4), "v"(line) : "memory");
      }
    }
#pragma nounroll
    while (!flag_one(fm + (W9 == 2 ? F_OD + 12 : F_OR) * 4)) NAP(W9NAP);
    finish(tag);
  } else {
    PRIO(3);
    s_m[lane] = 0;
    s_m[64 + lane] = 0;
    unsigned ep;
    asm volatile("s_waitcnt lgkmcnt(0)\n\ts_load_dword %0, %1, 0x0\n\ts_barrier\n\ts_waitcnt lgkmcnt(0)" : "=s"(ep) : "s"(ws + WS_EPOCH) : "memory");
    const unsigned tag = ep + 1;
    asm volatile("ds_write_b64 %0, %1" ::"v"(fm + F_TAG * 4), "v"(u32x2{tag, 1u}) : "memory");
#pragma nounroll
    while (flag_val(fm + F_CNT * 4) != 4) NAP(2);
    if (KA == 2) asm volatile("" ::"s"(wq), "s"(wo));
    STAMP(61);
    {
      auto put_q = [&](unsigned k, float e0, float e1) { ((unsigned*)(lds + L_Q))[k * 64 + lane] = pack2(e0, e1); };
      NAP(GNAP);
      float gq[4];
      FlyQ fly;
      pollq(fly, gq, lane * 16, ws + WS_Q + h * 8192 + bg * 4096, tag, SPIN_MAX);
      put_q(0, gq[0] + gq[2], gq[1] + gq[3]);
      flag_set(fm + F_SELOK * 4);
      STAMP(24);
      landed(fly);
      NAP(QNAP);
      if (j) {
#pragma nounroll
        for (unsigned spin = 0; spin < SPIN_MAX; spin++) {
          u32x2 g;
          ld8_dev<0>(g, (lane & 7) * 8, ws + WS_QF + h * 64);
          waitv<0>(g);
          if (__all(g[1] == tag)) break;
          NAP(16);
        }
        u32x2 ga, gb, gc, gd;
        const char* qb = ws + WS_QN + h * 16384 + (slab_of(j) >> 5) * 2048;
        ld8_dev<0>(ga, lane * 8, qb); ld8_dev<512>(gb, lane * 8, qb); ld8_dev<1024>(gc, lane * 8, qb); ld8_dev<1536>(gd, lane * 8, qb);
        waitv<0>(ga); waitv<0>(gb); waitv<0>(gc); waitv<0>(gd);
        const unsigned a0 = ga[0], a1 = ga[1], b0 = gb[0], b1_ = gb[1], c0 = gc[0], c1 = gc[1], d0 = gd[0], d1 = gd[1];
        const float e0 = (u2f(a0) + u2f(b0)) + (u2f(c0) + u2f(d0)), e1 = (u2f(a1) + u2f(b1_)) + (u2f(c1) + u2f(d1));
        put_q(j, e0, e1);
        st8_dev(ws + WS_QC + team * 2048, lane * 8 + j * 512, u32x2{pack2(e0, e1), tag});
      }
      if (QT) {
        NAP(QN2);
#pragma nounroll
        for (unsigned k = 1; k < 4; k++) {
          if (k == j) continue;
          u32x2 g;
#pragma nounroll
          for (unsigned spin = 0; spin < SPIN_MAX; spin++) {
            ld8_dev<0>(g, lane * 8 + k * 512, ws + WS_QC + team * 2048);
            waitv<0>(g);
            if (__all(g[1] == tag)) break;
            NAP(16);
          }
          ((unsigned*)(lds + L_Q))[k * 64 + lane] = g[0];
        }
      }
      flag_set(fm + F_QA * 4);
    }
#pragma nounroll
    while (flag_val(fm + F_WT * 4) != 8) NAP(8);
#pragma nounroll
    while (!flag_one(fm + F_SELV * 4)) NAP(2);
    const unsigned sk = flag_val(fm + F_SEL * 4) & 3, sv = flag_val(fm + F_SEL * 4) >> 2;
    const unsigned tstart = 0;
    PRIO(0);
    const bool same = sk == sv;
    u32x4 ml = {0, tag, 0, tag};
#pragma nounroll
    for (unsigned k = 0; k < 4; k++) {
#pragma nounroll
      while (!flag_four(fm + (F_KD + (same ? k : k | 1) * 4) * 4)) NAP(16);
      if (NOX != 1 && !same && !(k & 1)) {
        char* sb = SST ? ws + WS_S2 + (size_t)(k >> 1) * (16 * SST) + team * 1024 : ws + WS_S + (size_t)(team * 2 + (k >> 1)) * 16384;
        auto so = [=](unsigned cls, unsigned i) { return SST ? cls * 256 + (i * 4 + (lane >> 4)) * SST + (lane & 15) * 16 : cls * 4096 + i * 1024 + lane * 16; };
        auto sf = [=](unsigned cls) { return SST ? cls * 256 + 2 * NX * SST : cls * 4096 + 3072; };
        u32x4* sp[NX / 2];
        if (SW1) {
#pragma nounroll
          while (LWN ? !reached(fm + F_LINE * 4, k ? SL2 : SL1) : (int)((unsigned)__builtin_amdgcn_s_memrealtime() - tstart - (k ? SW2 : SW1)) < 0) NAP(WNAP);
        }
        PRIO(3);
#pragma unroll
        for (int i = 0; i < NX / 2; i++) {
          const unsigned c = i * 4 + (lane >> 4), ks = k + (c >= NX), cj = c >= NX ? c - NX : c;
          const unsigned u = ks ? ((XLIST >> (4 * cj)) - m) & 15 : NX == 4 ? 4 + x3 + 3 * cj : 4 + 2 * cj + (~m & 1);
          sp[i] = (u32x4*)(lds + L_SC + ks * 4096 + u * 256 + (lane & 15) * 16);
          if (NOX != 2 && NOX < 4) asm volatile("global_store_dwordx4 %0, %1, %2" SSP "\n\ts_nop 1" ::"v"(so(sk, i)), "v"(*sp[i]), "s"(sb) : "memory");
        }
        drain();
        if (lane == 0 && NOX != 5 && NOX != 7) { if (SFL) st8_dev(sb, sf(sk), u32x2{0, tag}); else st8_dev(ws + WS_SF, ((team * 2 + (k >> 1)) * 4 + sk) * 8, u32x2{0, tag}); }
#pragma nounroll
        for (unsigned spin = 0; spin < SPIN_MAX; spin++) {
          u32x2 g;
          if (NOX == 5 || NOX == 6) break;
          NAP(96);
          if (NOX == 7) { NAP(96); break; }
          if (SFL) ld8_dev(g, sf(sv), sb); else ld8_dev(g, ((team * 2 + (k >> 1)) * 4 + sv) * 8, ws + WS_SF);
          waitv<0>(g);
          if (__all(g[1] == tag)) break;
        }
        u32x4 f[NX / 2];
#pragma unroll
        for (int i = 0; i < NX / 2; i++)
          if (NOX >= 3) f[i] = *sp[i]; else asm volatile("global_load_dwordx4 %0, %1, %2" SLP : "=v"(f[i]) : "v"(so(sv, i)), "s"(sb) : "memory");
#pragma unroll
        for (int i = 0; i < NX / 2; i++) { waitv<0>(f[i]); *sp[i] = f[i]; }
        PRIO(0);
      }
      u32x4 v[4];
#pragma unroll
      for (int i = 0; i < 4; i++) v[i] = *(const u32x4*)(lds + L_SC + k * 4096 + i * 1024 + lane * 16);
      float m0 = -3.0e38f, mx = -3.0e38f, l0 = 0.f, l = 0.f;
#pragma unroll
      for (int e = 0; e < 4; e++) m0 = fmaxf(m0, fmaxf(bf_lo(v[0][e]), bf_hi(v[0][e])));
#pragma unroll
      for (int i = 1; i < 4; i++)
#pragma unroll
        for (int e = 0; e < 4; e++) mx = fmaxf(mx, fmaxf(bf_lo(v[i][e]), bf_hi(v[i][e])));
      m0 = wave_max(m0);
      mx = wave_max(mx);
      if (k) m0 = mx = fmaxf(m0, mx);
#pragma unroll
      for (int i = 0; i < 4; i++) {
        unsigned p[8];
        const float mi = i ? mx : m0;
#pragma unroll
        for (int e = 0; e < 4; e++) {
          const float pa = __builtin_amdgcn_exp2f((bf_lo(v[i][e]) - mi) * LOG2E), pb = __builtin_amdgcn_exp2f((bf_hi(v[i][e]) - mi) * LOG2E);
          if (i) l += pa + pb; else l0 += pa + pb;
          p[2 * e] = f2u(rbf(pa)); p[2 * e + 1] = f2u(rbf(pb));
        }
        unsigned* d = (unsigned*)(lds + L_P) + k * 2048 + i * 512 + lane * 8;
        *(u32x4*)d = u32x4{p[0], p[1], p[2], p[3]};
        *(u32x4*)(d + 4) = u32x4{p[4], p[5], p[6], p[7]};
      }
      flag_set(fm + (F_PR + k) * 4);
      l = wave_sum(k ? l + l0 : l);
      if (lane == (int)k) { ml[0] = f2u(mx); ml[2] = f2u(l); }
      if (k == 0) { l0 = wave_sum(l0); if (lane == 4) { ml[0] = f2u(m0); ml[2] = f2u(l0); } }
      if (k == 0 || k == 3) STAMP(32 + k);
    }
    char* ob = ws + WS_O + team * 4 * OB;
    auto own_o = [&](unsigned k, float fk) {
      float o0 = 0.f, o1 = 0.f;
#pragma unroll
      for (int w = 0; w < 4; w++) { const f32x2 t = *(const f32x2*)(lds + L_O + (k * 4 + w) * 512 + lane * 8); o0 += t[0]; o1 += t[1]; }
      return pack2(o0 * fk, o1 * fk);
    };
    float fac3;
    {
#pragma nounroll
      while (!flag_one(fm + F_LATE * 4) && (LWN ? !reached(fm + F_LINE * 4, OLW) : (int)((unsigned)__builtin_amdgcn_s_memrealtime() - tstart - OW) < 0)) NAP(WNAP);
#pragma nounroll
      while (!flag_four(fm + (F_OD + 8) * 4)) NAP(4);
      unsigned own[4];
      const float rl = 1.f / u2f(ml[2]);
#pragma unroll
      for (int k = 0; k < 4; k++) own[k] = own_o(k < 3 ? k : 4, u2f(__builtin_amdgcn_readlane(f2u(rl), k < 3 ? k : 4)));
      PRIO(3);
#pragma unroll
      for (int k = 0; k < 4; k += 2) {
        if (OL) {
          const unsigned na = __builtin_amdgcn_mov_dpp(own[k], 0xB1, 0xf, 0xf, true), nb = __builtin_amdgcn_mov_dpp(own[k + 1], 0xB1, 0xf, 0xf, true);
          const bool odd = lane & 1;
          st16_dev(ob, (odd ? (k ? ge * OB + 2048 : sv * OB + 512) : sv * OB + k * 512) + (lane & ~1) * 8, u32x4{odd ? nb : own[k], tag, odd ? own[k + 1] : na, tag});
        } else {
          st8_dev(ob, sv * OB + k * 512 + lane * 8, u32x2{own[k], tag});
          st8_dev(ob, (k ? ge * OB + 2048 : sv * OB + 512) + lane * 8, u32x2{own[k + 1], tag});
        }
      }
      if (lane < 5) st16_dev(ob, (lane < 4 ? sv : ge) * OB + 2560 + lane * 16, ml);
      STAMP(59);
      u32x2 g[13];
#pragma nounroll
      for (unsigned spin = 0; spin < SPIN_MAX; spin++) {
        NAP(32);
#pragma unroll
        for (int i = 0; i < 3; i++) {
          const unsigned o = (sv ^ (i + 1)) * OB + lane * 8;
          ld8_dev<0>(g[i], o, ob); ld8_dev<512>(g[3 + i], o, ob); ld8_dev<1024>(g[6 + i], o, ob);
          ld8_dev<2048>(g[9 + i], (ge ^ (i + 1)) * OB + lane * 8, ob);
        }
        ld8_dev<2560>(g[12], ((lane >> 1) & 3) * OB + (lane < 32 ? lane >> 3 : 4) * 16 + (lane & 1) * 8, ob);
        unsigned bad = 0;
#pragma unroll
        for (int i = 0; i < 13; i++) { waitv<0>(g[i]); bad |= g[i][1] ^ tag; }
        if (__all(bad == 0)) break;
      }
      const float val = u2f(g[12][0]), oth = dpp<0xB1>(val);
      const bool none = lane >= 40;
      const float mm = none ? -3.0e38f : (lane & 1) ? oth : val, ll = none ? 0.f : (lane & 1) ? val : oth;
      float M = fmaxf(mm, dpp<0x4E>(mm));
      M = fmaxf(M, dpp<0x141>(M));
      { const auto r = __builtin_amdgcn_permlane32_swap(f2u(M), f2u(M), false, false);
        const unsigned r0 = r[0], r1 = r[1];
        M = fmaxf(u2f(r0), u2f(r1)); }
      const float sc = __builtin_amdgcn_exp2f((mm - M) * LOG2E);
      float L = ll * sc;
      asm volatile("" : "+v"(L));
      const float ls = L;
      L += dpp<0x4E>(L);
      L += dpp<0x141>(L);
      L = fold32(L, L);
      const float wt = ls / L;
      fac3 = u2f(__builtin_amdgcn_readlane(f2u(sc / L), 24 + sv * 2));
      auto mix_o = [&](unsigned k, unsigned c, unsigned a0, unsigned a1, unsigned a2, unsigned a3) {
        const unsigned a[4] = {a0, a1, a2, a3};
        float lo[4], hi[4];
#pragma unroll
        for (unsigned i = 0; i < 4; i++) {
          const float w = u2f(__builtin_amdgcn_readlane(f2u(wt), k * 8 + (c ^ i) * 2));
          lo[i] = bf_lo(a[i]) * w; hi[i] = bf_hi(a[i]) * w;
          asm volatile("" : "+v"(lo[i]), "+v"(hi[i]));
        }
        return f32x2{(lo[0] + lo[1]) + (lo[2] + lo[3]), (hi[0] + hi[1]) + (hi[2] + hi[3])};
      };
#pragma unroll
      for (int k = 0; k < 3; k++) {
        f32x2 o = mix_o(k, sv, own[k], g[3 * k][0], g[3 * k + 1][0], g[3 * k + 2][0]);
        if (k == 0) o += mix_o(4, ge, own[3], g[9][0], g[10][0], g[11][0]);
        ((unsigned*)(lds + L_OB))[k * 64 + lane] = pack2(o[0], o[1]);
      }
      flag_set(fm + F_OR * 4);
      STAMP(30);
    }
    auto sum_o = [&](unsigned own, unsigned a, unsigned b, unsigned c) {
      return f32x2{(bf_lo(own) + bf_lo(a)) + (bf_lo(b) + bf_lo(c)), (bf_hi(own) + bf_hi(a)) + (bf_hi(b) + bf_hi(c))};
    };
    {
#pragma nounroll
      while (!flag_four(fm + (F_OD + 12) * 4)) NAP(1);
      const unsigned own = own_o(3, fac3);
      st8_dev(ob, sv * OB + 1536 + lane * 8, u32x2{own, tag});
      STAMP(60);
      Fly fly;
      unsigned got[3];
      const unsigned off[3] = {(sv ^ 1) * OB + lane * 8, (sv ^ 2) * OB + lane * 8, (sv ^ 3) * OB + lane * 8};
      poll3(fly, got, off, ob + 1536, tag, SPIN_MAX);
      const f32x2 o = sum_o(own, got[0], got[1], got[2]);
      ((unsigned*)(lds + L_OB))[192 + lane] = pack2(o[0], o[1]);
      flag_set(fm + (F_OR + 3) * 4);
      STAMP(31);
      landed(fly);
    }
  }
}

extern "C" void kernel_launch(void* const* d_in, const int* in_sizes, int n_in, void* d_out, int out_size, void* d_ws,
                              size_t ws_size, hipStream_t stream) {
  main_k<<<256, 640, 0, stream>>>((const unsigned short*)d_in[0], (const char*)d_in[1], (const char*)d_in[2],
                                  (const unsigned short*)d_in[3], (const unsigned short*)d_in[4],
                                  (const unsigned short*)d_in[6], (const unsigned short*)d_in[7],
                                  (const unsigned short*)d_in[8], (const unsigned short*)d_in[9], (char*)d_ws,
                                  (unsigned short*)d_out);
}
```
